```python
import torch

def _softmax(x, axis=-1):
    return torch.softmax(x, dim=axis)
def _gelu(x):
    return torch.nn.functional.gelu(x, approximate="tanh")
def _silu(x):
    return torch.nn.functional.silu(x)
def _sigmoid(x):
    return torch.sigmoid(x)
def _layernorm(x, axis=-1, eps=1e-5):
    m = x.mean(dim=axis, keepdim=True)
    v = x.var(dim=axis, keepdim=True, unbiased=False)
    return (x - m) / torch.sqrt(v + eps)
def _scatter_add(tgt, idx, src, axis=0):
    return tgt.clone().index_add_(axis, idx, src)
def _segment_sum(src, seg, n):
    out = torch.zeros((n,) + src.shape[1:], dtype=src.dtype)
    return out.index_add_(0, seg, src)
def _pad(x, pw):
    flat = tuple(v for pair in reversed(pw) for v in pair)
    return torch.nn.functional.pad(x, flat)

kernel_name = 'standalone__edge_featured_mpnn_gatedgcn__graph'

def setup_inputs():

    _w = (lambda a: (lambda t: t.to(torch.bfloat16) if t.is_floating_point() else t)(torch.as_tensor(a)))
    N, deg, D, De = 25000, 16, 128, max(16, 1)
    E = N * deg
    return {
        "x": _w((lambda *s: torch.randn(*s, dtype=torch.bfloat16) * 0.4)(N, D)),
        "e_attr": _w((lambda *s: torch.randn(*s, dtype=torch.bfloat16) * 0.4)(E, De)),
        "src": _w((lambda lo, hi, *s: torch.randint(lo, hi, s))(0, N, E).to(torch.int64)),
        "dst": _w((lambda lo, hi, *s: torch.randint(lo, hi, s))(0, N, E).to(torch.int64)),
        "w_a": _w((lambda *s: torch.randn(*s, dtype=torch.bfloat16) * 0.4)(D, D)),
        "w_b": _w((lambda *s: torch.randn(*s, dtype=torch.bfloat16) * 0.4)(D, D)),
        "w_c": _w((lambda *s: torch.randn(*s, dtype=torch.bfloat16) * 0.4)(De, D)),
        "w_u": _w((lambda *s: torch.randn(*s, dtype=torch.bfloat16) * 0.05)(D, D)),
        "w_v": _w((lambda *s: torch.randn(*s, dtype=torch.bfloat16) * 0.05)(D, D)),
    }

def reference(x, e_attr, src, dst, w_a, w_b, w_c, w_u, w_v):
    N, D = x.shape
    hv = x @ w_v
    g = x[dst] @ w_a + x[src] @ w_b + e_attr @ w_c
    eta = _sigmoid(g)
    denom = _segment_sum(eta, dst, N) + 1e-6
    eta_n = eta / denom[dst]
    agg = _segment_sum(eta_n * hv[src], dst, N)
    z = x @ w_u + agg
    relu_z = torch.where(z > 0, z, 0 * z)
    return x + relu_z
```

```cpp
#include <hip/hip_runtime.h>
#include <hip/hip_bf16.h>
#include <stdint.h>

static __device__ __forceinline__ float bf2f(uint16_t v){ return __uint_as_float(((uint32_t)v)<<16); }
static __device__ __forceinline__ uint16_t f2bf(float f){ uint32_t u=__float_as_uint(f); return (uint16_t)((u + 0x7FFF + ((u>>16)&1))>>16); }

__global__ void k_gemm(const uint16_t* x, const uint16_t* wa, const uint16_t* wb, const uint16_t* wu, const uint16_t* wv, float* T, int N){
  int n = blockIdx.x; int f = threadIdx.x & 127; int m = threadIdx.x >> 7;
  const uint16_t* w = m==0?wa:(m==1?wb:(m==2?wu:wv));
  float acc=0.f;
  for(int k=0;k<128;k++) acc += bf2f(x[n*128+k])*bf2f(w[k*128+f]);
  T[(size_t)n*512 + m*128 + f] = acc;
}
__global__ void k_hist(const int* dst, int* cnt, int E){ int e=blockIdx.x*256+threadIdx.x; if(e<E) atomicAdd(&cnt[dst[e]],1); }
__global__ void k_scan(const int* cnt, int* off, int* cur, int N){
  __shared__ int s[1024]; int t=threadIdx.x; int per=(N+1023)/1024; int sum=0;
  for(int i=0;i<per;i++){int j=t*per+i; if(j<N) sum+=cnt[j];}
  s[t]=sum; __syncthreads();
  for(int d=1;d<1024;d<<=1){int v=t>=d?s[t-d]:0; __syncthreads(); s[t]+=v; __syncthreads();}
  int base=s[t]-sum;
  for(int i=0;i<per;i++){int j=t*per+i; if(j<N){off[j]=base; cur[j]=base; base+=cnt[j];}}
  if(t==1023) off[N]=s[1023];
}
__global__ void k_scatter(const int* dst, int* cur, int* list, int E){ int e=blockIdx.x*256+threadIdx.x; if(e<E){int p=atomicAdd(&cur[dst[e]],1); list[p]=e;} }
__global__ void k_edge(const uint16_t* x, const uint16_t* ea, const int* src, const uint16_t* wc, const float* T, const int* off, const int* list, uint16_t* out){
  int n=blockIdx.x; int f=threadIdx.x;
  float w[16]; for(int k=0;k<16;k++) w[k]=bf2f(wc[k*128+f]);
  float a=T[(size_t)n*512+f]; float num=0,den=0;
  for(int p=off[n];p<off[n+1];p++){ int e=list[p]; int s=src[e];
    float c=0; for(int k=0;k<16;k++) c+=bf2f(ea[e*16+k])*w[k];
    float g=a+T[(size_t)s*512+128+f]+c; float eta=1.f/(1.f+__expf(-g));
    num+=eta*T[(size_t)s*512+384+f]; den+=eta; }
  float z=T[(size_t)n*512+256+f]+num/(den+1e-6f);
  out[n*128+f]=f2bf(bf2f(x[n*128+f])+(z>0?z:0.f));
}
extern "C" void kernel_launch(void* const* d_in, const int* in_sizes, int n_in, void* d_out, int out_size, void* d_ws, size_t ws_size, hipStream_t stream){
  const uint16_t* x=(const uint16_t*)d_in[0]; const uint16_t* ea=(const uint16_t*)d_in[1];
  const int* src=(const int*)d_in[2]; const int* dst=(const int*)d_in[3];
  const uint16_t* wa=(const uint16_t*)d_in[4]; const uint16_t* wb=(const uint16_t*)d_in[5]; const uint16_t* wc=(const uint16_t*)d_in[6];
  const uint16_t* wu=(const uint16_t*)d_in[7]; const uint16_t* wv=(const uint16_t*)d_in[8];
  int N=in_sizes[0]/128, E=in_sizes[2];
  char* ws=(char*)d_ws; int* cnt=(int*)ws; int* off=cnt+32768; int* cur=off+32768; int* list=cur+32768; float* T=(float*)(ws+(4<<20));
  hipMemsetAsync(cnt,0,32768*4,stream);
  k_gemm<<<N,512,0,stream>>>(x,wa,wb,wu,wv,T,N);
  k_hist<<<(E+255)/256,256,0,stream>>>(dst,cnt,E);
  k_scan<<<1,1024,0,stream>>>(cnt,off,cur,N);
  k_scatter<<<(E+255)/256,256,0,stream>>>(dst,cur,list,E);
  k_edge<<<N,128,0,stream>>>(x,ea,src,wc,T,off,list,(uint16_t*)d_out);
}
```

```cpp
#include <hip/hip_runtime.h>
#include <stdint.h>
#pragma clang diagnostic ignored "-Wint-to-pointer-cast"

#define EXP_NOLOAD 0
#define EXP_SAMEW 0
#define EXP_OVL 3
#define LAS __attribute__((address_space(3)))
typedef unsigned u32x2 __attribute__((ext_vector_type(2)));
typedef unsigned u32x4 __attribute__((ext_vector_type(4)));
typedef float f32x2 __attribute__((ext_vector_type(2)));
typedef float f32x16 __attribute__((ext_vector_type(16)));
typedef short s16x4 __attribute__((ext_vector_type(4)));
typedef __bf16 bf16x8 __attribute__((ext_vector_type(8)));
typedef __bf16 bf16x2 __attribute__((ext_vector_type(2)));

constexpr int NN = 25000, NE = 400000;
constexpr int NB = 98;
constexpr int FB = 49;
constexpr int CH = 1568;
constexpr int OFFP = 1040;
constexpr int WSTR = 25088 * 128;
constexpr int CAPT = 61;
constexpr float NLOG2E = -1.4426950408889634f;

constexpr size_t WS_TAB = 4096;
constexpr size_t WS_REC = WS_TAB + 4 * (size_t)WSTR;
constexpr size_t WS_OFF = WS_REC + 256 * CH * 4 + 4096;
constexpr size_t WS_WC = WS_OFF + 256 * OFFP + 4096;
constexpr size_t WS_DBG = WS_WC + 4096;

#define STAMP(k, i)
#define STAMPW(k, i)

__device__ __forceinline__ u32x4 make_srd(const void* p, unsigned bytes) {
  const uint64_t a = (uint64_t)p;
  return u32x4{(unsigned)a, (unsigned)(a >> 32) & 0xffffu, bytes, 0x00020000u};
}
__device__ __forceinline__ void dma16(u32x4 srd, unsigned voff, unsigned lds_dst) {
  unsigned keep;
  asm volatile("s_mov_b32 %0, m0\n\ts_mov_b32 m0, %3\n\ts_nop 0\n\tbuffer_load_dwordx4 %1, %2, 0 offen lds\n\ts_mov_b32 m0, %0"
               : "=&s"(keep) : "v"(voff), "s"(srd), "s"(lds_dst) : "memory");
}
__device__ __forceinline__ void bar_lds() { asm volatile("s_waitcnt lgkmcnt(0)\n\ts_barrier" ::: "memory"); }
__device__ __forceinline__ void bar_all() { asm volatile("s_waitcnt vmcnt(0) lgkmcnt(0)\n\ts_barrier" ::: "memory"); }
template <class T> __device__ __forceinline__ T ldsr(unsigned a) { return *(const LAS T*)a; }
template <class T> __device__ __forceinline__ void ldsw(unsigned a, T v) { *(LAS T*)a = v; }
__device__ __forceinline__ unsigned lds_add_rtn(unsigned a, unsigned v) {
  return __hip_atomic_fetch_add((LAS unsigned*)a, v, __ATOMIC_RELAXED, __HIP_MEMORY_SCOPE_WORKGROUP);
}
__device__ __forceinline__ void lds_addf(unsigned a, float v) {
  __hip_atomic_fetch_add((LAS float*)a, v, __ATOMIC_RELAXED, __HIP_MEMORY_SCOPE_WORKGROUP);
}
__device__ __forceinline__ unsigned lds_poll(unsigned a) {
  const unsigned v = __builtin_amdgcn_readfirstlane(__hip_atomic_load((LAS unsigned*)a, __ATOMIC_RELAXED, __HIP_MEMORY_SCOPE_WORKGROUP));
  asm volatile("" ::: "memory");
  return v;
}
__device__ __forceinline__ void lds_wait_ge(unsigned a, unsigned n) {
  for (int i = 0; i < (1 << 20) && lds_poll(a) < n; ++i) __builtin_amdgcn_s_sleep(1);
}
__device__ __forceinline__ int lds_inc_rtn(unsigned a) {
  unsigned r;
  asm volatile("s_mov_b64 exec, 1\n\tds_add_rtn_u32 %0, %1, %2\n\ts_mov_b64 exec, -1\n\ts_waitcnt lgkmcnt(0)" : "=&v"(r) : "v"(a), "v"(1u) : "memory");
  return __builtin_amdgcn_readfirstlane(r);
}
__device__ __forceinline__ void lds_inc(unsigned a) {
  asm volatile("s_mov_b64 exec, 1\n\tds_add_u32 %0, %1\n\ts_mov_b64 exec, -1" :: "v"(a), "v"(1u) : "memory");
}
__device__ __forceinline__ u32x2 tr_read(unsigned a) {
  return __builtin_bit_cast(u32x2, __builtin_amdgcn_ds_read_tr16_b64_v4i16((LAS s16x4*)a));
}
__device__ __forceinline__ unsigned cvt_pk(float lo, float hi) {
  return __builtin_bit_cast(unsigned, bf16x2{(__bf16)lo, (__bf16)hi});
}
__device__ __forceinline__ bf16x2 as_bf2(unsigned v) { return __builtin_bit_cast(bf16x2, v); }
__device__ __forceinline__ f32x16 mfma(u32x4 a, u32x4 b, f32x16 c) {
  return __builtin_amdgcn_mfma_f32_32x32x16_bf16(__builtin_bit_cast(bf16x8, a), __builtin_bit_cast(bf16x8, b), c, 0, 0, 0);
}
__device__ __forceinline__ unsigned wave_incl_scan(unsigned v) {
  v += __builtin_amdgcn_update_dpp(0u, v, 0x111, 0xf, 0xf, true);
  v += __builtin_amdgcn_update_dpp(0u, v, 0x112, 0xf, 0xf, true);
  v += __builtin_amdgcn_update_dpp(0u, v, 0x114, 0xf, 0xf, true);
  v += __builtin_amdgcn_update_dpp(0u, v, 0x118, 0xf, 0xf, true);
  v += __builtin_amdgcn_update_dpp(0u, v, 0x142, 0xa, 0xf, true);
  v += __builtin_amdgcn_update_dpp(0u, v, 0x143, 0xc, 0xf, true);
  return v;
}

__device__ __forceinline__ void stage_wx(const void* w0, const void* w1, const void* x, int node0, unsigned ldsW, unsigned ldsX, int wv, int lane) {
  const unsigned m = ((lane >> 4) << 2) | (wv & 3);
  const unsigned voff = (4 * wv + (lane >> 4)) * 256 + 16 * ((lane & 15) ^ m);
  const u32x4 sx = make_srd(x, NN * 256), s0 = make_srd(w0, 32768), s1 = make_srd(w1, 32768);
  dma16(sx, node0 * 256 + voff, ldsX + wv * 1024);
  if (wv < 9) { if (wv < 8 || lane < 32) dma16(sx, node0 * 256 + voff + 16384, ldsX + 16384 + wv * 1024); }
  dma16(s0, voff, ldsW + wv * 1024);
  dma16(s0, voff + 16384, ldsW + 16384 + wv * 1024);
  dma16(s1, voff, ldsW + 32768 + wv * 1024);
  dma16(s1, voff + 16384, ldsW + 49152 + wv * 1024);
}
__device__ __forceinline__ void gemm_tile(unsigned ldsW, unsigned ldsX, int rt, int cb, int lane, f32x16& c0, f32x16& c1) {
  const unsigned h = lane >> 5, blk = (lane >> 4) & 1, q = (lane & 15) >> 2, p = lane & 3;
  const unsigned row = 32 * rt + (lane & 31);
  const unsigned xa = ldsX + 256 * row + 16 * (h ^ (((row & 3) << 2) | ((row >> 2) & 3)));
  const unsigned ch = 4 * cb + 2 * blk + (p >> 1);
  const unsigned wa0 = ldsW + 256 * (8 * h + q) + 16 * (ch ^ ((q << 2) | (2 * h))) + 8 * (p & 1);
  const unsigned wa1 = ldsW + 256 * (8 * h + 4 + q) + 16 * (ch ^ ((q << 2) | (2 * h + 1))) + 8 * (p & 1);
#pragma unroll
  for (int i = 0; i < 16; ++i) { c0[i] = 0.f; c1[i] = 0.f; }
#pragma unroll
  for (int s = 0; s < 8; ++s) {
    const u32x4 a = ldsr<u32x4>(xa ^ (32 * s));
    const u32x2 b00 = tr_read(wa0 + 4096 * s), b01 = tr_read(wa1 + 4096 * s);
    const u32x2 b10 = tr_read(wa0 + 4096 * s + 32768), b11 = tr_read(wa1 + 4096 * s + 32768);
    c0 = mfma(a, u32x4{b00[0], b00[1], b01[0], b01[1]}, c0);
    c1 = mfma(a, u32x4{b10[0], b10[1], b11[0], b11[1]}, c1);
  }
}

constexpr unsigned L1_W = 0, L1_X = 65536, L1_CNT = 98304, L1_OFF = L1_CNT + 2048, L1_REC = L1_OFF + 1024, L1_END = L1_REC + CH * 4;

__global__ void __launch_bounds__(1024) k1_prep(const uint16_t* __restrict__ x, const int* __restrict__ src, const int* __restrict__ dst,
                                                const uint16_t* __restrict__ wb, const uint16_t* __restrict__ wv_, const uint16_t* __restrict__ wc,
                                                char* __restrict__ ws) {
  __shared__ __attribute__((aligned(256))) char smem[L1_END];
  const unsigned sb = (unsigned)(size_t)(LAS char*)smem;
  const int tid = threadIdx.x, lane = tid & 63, wv = __builtin_amdgcn_readfirstlane(tid >> 6);
  const int g = blockIdx.x;
  const int node0 = ((g & 7) * 32 + (g >> 3)) * NB;
  STAMP(0, 0);
  const int bt = tid - 512;
  const bool bin = bt >= 0 && bt < CH / 4 && g * CH + 4 * bt < NE;
  u32x4 d4 = {0, 0, 0, 0}, s4 = {0, 0, 0, 0};
  if (bin) { d4 = *(const u32x4*)(dst + g * CH + 4 * bt); s4 = *(const u32x4*)(src + g * CH + 4 * bt); }
  stage_wx(wb, wv_, x, node0, sb + L1_W, sb + L1_X, wv, lane);
  if (tid < 512) ldsw<unsigned>(sb + L1_CNT + 4 * tid, 0u);
  if (wv == 15 && g < 4) {
    unsigned f[4];
#pragma unroll
    for (int j = 0; j < 4; ++j) {
      const int k = 8 * (lane >> 5) + 2 * j, c = 32 * g + (lane & 31);
      const float a = __uint_as_float((unsigned)wc[k * 128 + c] << 16), b = __uint_as_float((unsigned)wc[k * 128 + 128 + c] << 16);
      f[j] = cvt_pk(a * NLOG2E, b * NLOG2E);
    }
    *(u32x4*)(ws + WS_WC + g * 1024 + lane * 16) = u32x4{f[0], f[1], f[2], f[3]};
    if (lane < 32) *(unsigned*)(ws + WS_TAB + (size_t)g * WSTR + NN * 128 + 4 * lane) = 0x00007f00u;
  }
  bar_lds();
  unsigned fine[4], rank[4];
  if (bin) {
#pragma unroll
    for (int j = 0; j < 4; ++j) { fine[j] = d4[j] / FB; rank[j] = lds_add_rtn(sb + L1_CNT + 4 * fine[j], 1u); }
  }
  bar_all();
  STAMP(0, 1);
  if (wv >= 8 && wv < 15) {
    const u32x4 a = ldsr<u32x4>(sb + L1_CNT + 32 * lane), b = ldsr<u32x4>(sb + L1_CNT + 32 * lane + 16);
    const unsigned p1 = a[0], p2 = p1 + a[1], p3 = p2 + a[2], p4 = p3 + a[3], p5 = p4 + b[0], p6 = p5 + b[1], p7 = p6 + b[2], tot = p7 + b[3];
    const unsigned inc = wave_incl_scan(tot), e = inc - tot;
    const u32x4 o = {e | ((e + p1) << 16), (e + p2) | ((e + p3) << 16), (e + p4) | ((e + p5) << 16), (e + p6) | ((e + p7) << 16)};
    ldsw<u32x4>(sb + L1_OFF + 16 * lane, o);
    if (wv == 8) {
      *(u32x4*)(ws + WS_OFF + g * OFFP + 16 * lane) = o;
      if (lane == 63) *(unsigned*)(ws + WS_OFF + g * OFFP + 1024) = inc;
    }
    if (bin) {
#pragma unroll
      for (int j = 0; j < 4; ++j) {
        const unsigned pos = ldsr<uint16_t>(sb + L1_OFF + 2 * fine[j]) + rank[j];
        ldsw<unsigned>(sb + L1_REC + 4 * pos, (unsigned)(4 * bt + j) | (s4[j] << 11) | ((d4[j] - fine[j] * FB) << 26));
      }
    }
  }
  {
    const int rt = wv & 3, cb = wv >> 2;
    f32x16 cB, cV;
    gemm_tile(sb + L1_W, sb + L1_X, rt, cb, lane, cB, cV);
    STAMP(0, 2);
    char* tab = ws + WS_TAB + (size_t)cb * WSTR + (size_t)(node0 + 32 * rt + 4 * (lane >> 5)) * 128 + (lane & 31) * 4;
    const int lim = min(NB, NN - node0) - 32 * rt - 4 * (lane >> 5);
#pragma unroll
    for (int r = 0; r < 16; ++r) {
      const int rr = (r & 3) + 8 * (r >> 2);
      const unsigned lo = cvt_pk(cB[r] * NLOG2E, 0.f);
      const int u = __float_as_int(cV[r]);
      int t = __builtin_elementwise_add_sat(u, (int)(0x8000u - lo));
      t = max(t, u & (int)0x80000000);
      if (rr < lim) *(unsigned*)(tab + rr * 128) = ((unsigned)t & 0xffff0000u) | lo;
    }
  }
  bar_lds();
  if (bin) *(u32x4*)(ws + WS_REC + (size_t)(g * CH + 4 * bt) * 4) = ldsr<u32x4>(sb + L1_REC + 16 * bt);
  STAMP(0, 3);
}

constexpr unsigned L2_A = 0, L2_UX = 99 * 256, L2_WC = L2_UX + 99 * 512,
                   L2_W = 0, L2_X = 65536,
                   L2_E = L2_X + NB * 256, L2_SRC = L2_E + CAPT * 1024, L2_CNT = L2_SRC + CAPT * 128, L2_HIST = L2_CNT + 400,
                   L2_SRT = L2_HIST + 256, L2_MISC = L2_SRT + 400, L2_NI = L2_MISC + 16, L2_GI = L2_NI + 400, L2_GN = L2_GI + 112,
                   L2_END = L2_GN + 208;
static_assert(L2_END <= 163840 && L2_WC + 4096 <= L2_E && L2_SRT % 16 == 0 && L2_GN % 8 == 0, "LDS map");
constexpr unsigned M_NEXT = L2_MISC, M_SLOW = L2_MISC + 8, M_INFO = L2_MISC + 12;
constexpr int NG = 25, OVL = EXP_OVL, NITEMS = 4 * NG;

struct Item { int w, t; };
__device__ __forceinline__ Item item_of(int i) {
  int w = i >= 2 * NG ? 2 : 0, r = i >= 2 * NG ? i - 2 * NG : i, m = -1;
  if (r >= NG) { r -= NG; w += 1; }
  if (r >= NG - OVL && w < 3) m = r - (NG - OVL);
  else if (r < OVL && w > 0) { m = r + OVL; w -= 1; }
  if (m >= 0) { if (m & 1) { w += 1; r = m >> 1; } else r = NG - OVL + (m >> 1); }
  return Item{w, r};
}
struct Tile { int ps, np, w, g, fl; };

#define DOT2(d, w, s, a) "v_dot2_f32_bf16 " d ", " w ", " s ", " a "\n\t"

__global__ void __launch_bounds__(1024) k2_edge(const uint16_t* __restrict__ x, const uint16_t* __restrict__ ea,
                                                const uint16_t* __restrict__ wa, const uint16_t* __restrict__ wu,
                                                char* __restrict__ ws, uint16_t* __restrict__ out) {
  __shared__ __attribute__((aligned(256))) char smem[L2_END];
  const unsigned sb = (unsigned)(size_t)(LAS char*)smem;
  const int tid = threadIdx.x, lane = tid & 63, wv = __builtin_amdgcn_readfirstlane(tid >> 6);
  const int bk = (blockIdx.x & 7) * 32 + (blockIdx.x >> 3), node0 = bk * NB;
  const int h = lane >> 5, c = lane & 31;
  STAMP(1, 0);
  const int seg = tid >> 2, sj = tid & 3;
  const u32x2 o2 = *(const u32x2*)(ws + WS_OFF + seg * OFFP + 4 * bk);
  if (tid < (L2_NI - L2_CNT) / 4) {
    const unsigned a = L2_CNT + 4 * tid;
    ldsw<unsigned>(sb + a, a == M_NEXT ? 16u : (a >= L2_SRT && a < L2_MISC) ? (unsigned)NB : 0u);
  }
  if (tid < CAPT * 8) {
    const unsigned dv = NN * 128 + 128 - 128 * (tid & 1);
    ldsw<u32x4>(sb + L2_SRC + 16 * tid, u32x4{dv, dv, dv, dv});
    ldsw<u32x4>(sb + L2_E + (tid >> 3) * 1024 + (tid & 7) * 16, u32x4{0, 0, 0, 0});
  }
  bar_lds();
  const int s0 = o2[0] & 0xffff, s1 = o2[0] >> 16, len = (o2[1] & 0xffff) - s0;
  const unsigned* recs = (const unsigned*)(ws + WS_REC) + seg * CH + s0;
  const bool big = __any(len > 16);
  u32x4 ra = {0, 0, 0, 0}, rb = {0, 0, 0, 0};
  if (4 * sj < len) ra = *(const u32x4*)(recs + 4 * sj);
  if (big) { if (16 + 4 * sj < len) rb = *(const u32x4*)(recs + 16 + 4 * sj); if (len > 32) ldsw<unsigned>(sb + M_SLOW, 1u); }
  STAMP(1, 1);
  unsigned na[4], ka[4], nb_[4], kb[4];
#pragma unroll
  for (int j = 0; j < 4; ++j) {
    const int i = 4 * sj + j;
    na[j] = (ra[j] >> 26) + (s0 + i >= s1 ? FB : 0); ka[j] = 0;
    if (i < len) ka[j] = lds_add_rtn(sb + L2_CNT + 4 * na[j], 1u);
  }
  if (big) {
#pragma unroll
    for (int j = 0; j < 4; ++j) {
      const int i = 16 + 4 * sj + j;
      nb_[j] = (rb[j] >> 26) + (s0 + i >= s1 ? FB : 0); kb[j] = 0;
      if (i < len) kb[j] = lds_add_rtn(sb + L2_CNT + 4 * nb_[j], 1u);
    }
  }
  u32x4 wcv = {0, 0, 0, 0};
  if (wv >= 12) wcv = *(const u32x4*)(ws + WS_WC + (tid - 768) * 16);
  stage_wx(wa, wu, x, node0, sb + L2_W, sb + L2_X, wv, lane);
  bar_lds();
  STAMP(1, 2);
  if (wv == 0) {
    unsigned k0 = 0, k1 = 0, b0 = 0, b1 = 0, r0 = 0, r1 = 0;
    if (lane < NB / 2) {
      const u32x2 cn = ldsr<u32x2>(sb + L2_CNT + 8 * lane);
      k0 = (cn[0] + 1) >> 1; k1 = (cn[1] + 1) >> 1;
      b0 = 63 - min(k0, 63u); b1 = 63 - min(k1, 63u);
      r0 = lds_add_rtn(sb + L2_HIST + 4 * b0, 1u); r1 = lds_add_rtn(sb + L2_HIST + 4 * b1, 1u);
    }
    const unsigned hv = __hip_atomic_load((LAS unsigned*)(sb + L2_HIST + 4 * lane), __ATOMIC_RELAXED, __HIP_MEMORY_SCOPE_WORKGROUP);
    const unsigned hb = wave_incl_scan(hv) - hv;
    const unsigned p0 = __builtin_amdgcn_ds_bpermute(4 * b0, hb) + r0, p1 = __builtin_amdgcn_ds_bpermute(4 * b1, hb) + r1;
    if (lane < NB / 2) { ldsw<unsigned>(sb + L2_SRT + 4 * p0, (2 * lane) | (k0 << 8)); ldsw<unsigned>(sb + L2_SRT + 4 * p1, (2 * lane + 1) | (k1 << 8)); }
    asm volatile("" ::: "memory");
    u32x4 q = {0, 0, 0, 0};
    unsigned gl = 0;
    if (lane < NG) { q = ldsr<u32x4>(sb + L2_SRT + 16 * lane); gl = max(max(max(q[0], q[1]), max(q[2], q[3])) >> 8, 1u); }
    const unsigned inc = wave_incl_scan(gl), gb = inc - gl;
    if (lane < NG) {
      ldsw<unsigned>(sb + L2_GI + 4 * lane, gb | (gl << 16));
      ldsw<u32x2>(sb + L2_GN + 8 * lane, u32x2{((q[0] & 255) << 8) | ((q[1] & 255) << 24), ((q[2] & 255) << 8) | ((q[3] & 255) << 24)});
#pragma unroll
      for (int j = 0; j < 4; ++j) ldsw<unsigned>(sb + L2_NI + 4 * (q[j] & 255), gb * 8 + (j >> 1) * 4 + (j & 1));
    }
    if (lane == 63) { ldsw<unsigned>(sb + M_INFO, inc); if (inc > CAPT * 4) ldsw<unsigned>(sb + M_SLOW, 1u); }
  }
  bar_lds();
  const u32x2 inf = ldsr<u32x2>(sb + M_SLOW);
  const bool slow = __builtin_amdgcn_readfirstlane(inf[0]) != 0;
  const int nte = slow ? 0 : (__builtin_amdgcn_readfirstlane(inf[1]) + 3) >> 2;
  auto place = [&](unsigned rec, unsigned node, unsigned k) __attribute__((always_inline)) {
    const unsigned L = ldsr<unsigned>(sb + L2_NI + 4 * node) + (k >> 1) * 8 + (k & 1) * 2;
    ldsw<unsigned>(sb + L2_SRC + 4 * L, ((rec >> 11) & 0x7fff) * 128 + 128 - 32 * (L & 4));
    ldsw<unsigned>(sb + L2_E + (L >> 5) * 1024 + (L & 31) * 4, (seg * CH + (rec & 0x7ff)) * 32);
  };
  if (!slow) {
#pragma unroll
    for (int j = 0; j < 4; ++j) if (4 * sj + j < len) place(ra[j], na[j], ka[j]);
    if (big) {
#pragma unroll
      for (int j = 0; j < 4; ++j) if (16 + 4 * sj + j < len) place(rb[j], nb_[j], kb[j]);
    }
  }
  bar_all();
  STAMP(1, 3);
  {
    const u32x4 se = make_srd(ea, NE * 32);
    unsigned eo[4];
#pragma unroll
    for (int j = 0; j < 4; ++j) { eo[j] = 0; if (wv + 16 * j < nte) eo[j] = ldsr<unsigned>(sb + L2_E + (wv + 16 * j) * 1024 + 4 * (lane >> 1)) + 16 * (lane & 1); }
#pragma unroll
    for (int j = 0; j < 4; ++j) if (wv + 16 * j < nte) dma16(se, eo[j], sb + L2_E + (wv + 16 * j) * 1024);
  }
  int s_ps = 0, s_rem = 0, s_w = 0, s_g = 0;
  auto open_item = [&](int idx) __attribute__((always_inline)) {
    const Item it = item_of(min(idx, NITEMS - 1));
    const unsigned gi = __builtin_amdgcn_readfirstlane(ldsr<unsigned>(sb + L2_GI + 4 * it.t));
    s_w = it.w; s_g = it.t; s_ps = gi & 0xffff; s_rem = idx < NITEMS ? gi >> 16 : 0;
  };
  auto take = [&](int fl) __attribute__((always_inline)) {
    const int np = min(s_rem, 4);
    const Tile t = {s_ps, np, s_w, s_g, fl | (s_rem == np ? 2 : 0)};
    s_ps += np; s_rem -= np;
    return t;
  };
  auto advance = [&]() __attribute__((always_inline)) {
    int fl = 0;
    if (s_rem == 0) { open_item(lds_inc_rtn(sb + M_NEXT)); fl = 1; }
    return take(fl);
  };
  auto issue = [&](const Tile& t, unsigned (&v)[16]) __attribute__((always_inline)) {
    const unsigned la = sb + L2_SRC + t.ps * 32 + h * 16;
    const int so = EXP_SAMEW ? 0 : t.w * WSTR;
#pragma unroll
    for (int u = 0; u < 4; ++u) {
      const auto rs = __builtin_amdgcn_make_buffer_rsrc(ws + WS_TAB - 128, (short)4, (u < t.np && !EXP_NOLOAD) ? 4 * WSTR + 128 : 0, 1 << 23);
      const u32x4 q = ldsr<u32x4>(la + 32 * u);
#pragma unroll
      for (int z = 0; z < 4; ++z) v[4 * u + z] = __builtin_amdgcn_raw_buffer_load_b32(rs, q[z], so, 0);
    }
  };
  unsigned va[16], vb[16];
  open_item(slow ? NITEMS : wv);
  if (slow) s_ps = 0;
  Tile ta = take(1);
  issue(ta, va);
  {
    const int rt = wv & 3, ow = wv >> 2;
    f32x16 cA, cU;
    gemm_tile(sb + L2_W, sb + L2_X, rt, ow, lane, cA, cU);
    const unsigned q = (lane & 15) >> 2, p = lane & 3, ch = 4 * ow + 2 * ((lane >> 4) & 1) + (p >> 1);
    unsigned ux[16];
#pragma unroll
    for (int g = 0; g < 4; ++g) {
      const unsigned row = 32 * rt + 8 * g + 4 * h + q;
      const u32x2 xx = tr_read(sb + L2_X + 256 * row + 16 * (ch ^ ((q << 2) | ((2 * g + h) & 3))) + 8 * (p & 1));
      const unsigned u01 = cvt_pk(cU[4 * g], cU[4 * g + 1]), u23 = cvt_pk(cU[4 * g + 2], cU[4 * g + 3]);
      ux[4 * g] = __builtin_amdgcn_perm(xx[0], u01, 0x05040100); ux[4 * g + 1] = __builtin_amdgcn_perm(xx[0], u01, 0x07060302);
      ux[4 * g + 2] = __builtin_amdgcn_perm(xx[1], u23, 0x05040100); ux[4 * g + 3] = __builtin_amdgcn_perm(xx[1], u23, 0x07060302);
    }
    bar_lds();
    STAMP(1, 4);
    const unsigned ab = (32 * rt + 4 * h) * 256 + (32 * ow + c) * 2;
    auto put_a = [&](int r) __attribute__((always_inline)) {
      const int rr = (r & 3) + 8 * (r >> 2);
      ldsw<uint16_t>(sb + L2_A + ab + rr * 256, (uint16_t)cvt_pk(cA[r] * NLOG2E, 0.f));
      ldsw<unsigned>(sb + L2_UX + 2 * ab + rr * 512, ux[r]);
    };
    if (rt < 3 || h == 0) { put_a(0); put_a(1); }
    if (rt < 3) {
#pragma unroll
      for (int r = 2; r < 16; ++r) put_a(r);
    }
    if (wv >= 12) ldsw<u32x4>(sb + L2_WC + (tid - 768) * 16, wcv);
  }
  asm volatile("s_waitcnt vmcnt(16) lgkmcnt(0)\n\ts_barrier" ::: "memory");
  STAMP(1, 5);

  const auto ro = __builtin_amdgcn_make_buffer_rsrc(out + (size_t)node0 * 128, (short)0, min(NB, NN - node0) * 256, 0x00020000);
  constexpr float EPS = 1e-6f * 0x1p30f;
  if (slow) {
    for (int w = 0; w < 4; ++w) {
      for (int i = tid; i < NB * 64; i += 1024) ldsw<unsigned>(sb + L2_E + 4 * i, 0u);
      bar_lds();
      const u32x4 w0 = ldsr<u32x4>(sb + L2_WC + w * 1024 + c * 16), w1 = ldsr<u32x4>(sb + L2_WC + w * 1024 + 512 + c * 16);
      for (int sg = wv; sg < 256; sg += 16) {
        const u32x2 oo = *(const u32x2*)(ws + WS_OFF + sg * OFFP + 4 * bk);
        const int b0 = oo[0] & 0xffff, b1 = oo[0] >> 16, b2 = oo[1] & 0xffff;
        for (int i = b0 + h; i < b2; i += 2) {
          const unsigned rec = ((const unsigned*)(ws + WS_REC))[sg * CH + i];
          const unsigned node = (rec >> 26) + (i >= b1 ? FB : 0), sn = (rec >> 11) & 0x7fff, eid = sg * CH + (rec & 0x7ff);
          const u32x4 e0 = *(const u32x4*)(ea + eid * 16), e1 = *(const u32x4*)(ea + eid * 16 + 8);
          const unsigned wd = *(const unsigned*)(ws + WS_TAB + (size_t)w * WSTR + sn * 128 + 4 * c);
          float gs = __uint_as_float((unsigned)ldsr<uint16_t>(sb + L2_A + node * 256 + (32 * w + c) * 2) << 16) + __uint_as_float(wd << 16);
#pragma unroll
          for (int j = 0; j < 4; ++j) {
            gs = __builtin_amdgcn_fdot2_f32_bf16(as_bf2(e0[j]), as_bf2(w0[j]), gs, false);
            gs = __builtin_amdgcn_fdot2_f32_bf16(as_bf2(e1[j]), as_bf2(w1[j]), gs, false);
          }
          const float eta = __builtin_amdgcn_rcpf(fmaf(__builtin_amdgcn_exp2f(gs), 0x1p-30f, 0x1p-30f));
          lds_addf(sb + L2_E + node * 256 + 4 * c, eta * __uint_as_float(wd)); lds_addf(sb + L2_E + node * 256 + 128 + 4 * c, eta);
        }
      }
      bar_lds();
      for (int n = 2 * wv + h; n < NB; n += 32) {
        const unsigned o = n * 256 + (32 * w + c) * 2, ux = ldsr<unsigned>(sb + L2_UX + 2 * o);
        const float z = fmaf(ldsr<float>(sb + L2_E + n * 256 + 4 * c), __builtin_amdgcn_rcpf(ldsr<float>(sb + L2_E + n * 256 + 128 + 4 * c) + EPS), __uint_as_float(ux << 16));
        __builtin_amdgcn_raw_buffer_store_b16((uint16_t)cvt_pk(__uint_as_float(ux & 0xffff0000u) + fmaxf(z, 0.f), 0.f), ro, o, 0, 0);
      }
      bar_lds();
    }
    return;
  }

  const unsigned one_lo = 0x00003f80u;
  unsigned o0 = 0, o1 = 0;
  f32x2 A2 = {0.f, 0.f}, num = {0.f, 0.f}, den = {0.f, 0.f};
  u32x4 wf = {0, 0, 0, 0};
  auto compute = [&](const Tile& t, unsigned (&v)[16]) __attribute__((always_inline)) {
    if (t.np == 0) return;
    const u32x4 ef = ldsr<u32x4>(sb + L2_E + t.ps * 256 + c * 32 + h * 16);
    if (t.fl & 1) {
      const unsigned nd = ldsr<unsigned>(sb + L2_GN + 8 * t.g + 4 * h), fo = (32 * t.w + c) * 2;
      wf = ldsr<u32x4>(sb + L2_WC + t.w * 1024 + lane * 16);
      o0 = (nd & 0xffff) + fo; o1 = (nd >> 16) + fo;
      A2 = f32x2{__uint_as_float((unsigned)ldsr<uint16_t>(sb + L2_A + o0) << 16), __uint_as_float((unsigned)ldsr<uint16_t>(sb + L2_A + o1) << 16)} - 60.f;
      num = f32x2{0.f, 0.f}; den = f32x2{0.f, 0.f};
    }
    float d[16];
#pragma unroll
    for (int u = 0; u < 4; ++u) {
      if (u == 0 || u < t.np)
        asm(DOT2("%0", "%4", "%8", "%9") DOT2("%1", "%5", "%8", "%10") DOT2("%2", "%6", "%8", "%9") DOT2("%3", "%7", "%8", "%10")
            : "=&v"(d[4 * u]), "=&v"(d[4 * u + 1]), "=&v"(d[4 * u + 2]), "=&v"(d[4 * u + 3])
            : "v"(v[4 * u]), "v"(v[4 * u + 1]), "v"(v[4 * u + 2]), "v"(v[4 * u + 3]), "s"(one_lo), "v"(A2[0]), "v"(A2[1]));
      else
        asm("" : "=v"(d[4 * u]), "=v"(d[4 * u + 1]), "=v"(d[4 * u + 2]), "=v"(d[4 * u + 3]));
    }
    f32x16 g;
#pragma unroll
    for (int r = 0; r < 16; ++r) g[r] = d[r];
    asm volatile("s_nop 2" : "+v"(g));
    g = mfma(ef, wf, g);
#pragma unroll
    for (int u = 0; u < 4; ++u) {
      if (u == 0 || u < t.np) {
        const f32x2 t0 = {__builtin_amdgcn_fmed3f(__builtin_amdgcn_exp2f(g[4 * u]), 0.f, 1.f), __builtin_amdgcn_fmed3f(__builtin_amdgcn_exp2f(g[4 * u + 1]), 0.f, 1.f)};
        const f32x2 t1 = {__builtin_amdgcn_fmed3f(__builtin_amdgcn_exp2f(g[4 * u + 2]), 0.f, 1.f), __builtin_amdgcn_fmed3f(__builtin_amdgcn_exp2f(g[4 * u + 3]), 0.f, 1.f)};
        const f32x2 a0 = __builtin_elementwise_fma(t0, f32x2{0x1p30f, 0x1p30f}, f32x2{0x1p-30f, 0x1p-30f});
        const f32x2 a1 = __builtin_elementwise_fma(t1, f32x2{0x1p30f, 0x1p30f}, f32x2{0x1p-30f, 0x1p-30f});
        const f32x2 v0 = {__uint_as_float(v[4 * u]), __uint_as_float(v[4 * u + 1])}, v1 = {__uint_as_float(v[4 * u + 2]), __uint_as_float(v[4 * u + 3])};
        const f32x2 p = a0 * a1, r = {__builtin_amdgcn_rcpf(p[0]), __builtin_amdgcn_rcpf(p[1])};
        num = __builtin_elementwise_fma(__builtin_elementwise_fma(v1, a0, v0 * a1), r, num);
        den = __builtin_elementwise_fma(a0 + a1, r, den);
      }
    }
    if (t.fl & 2) {
      const unsigned x0 = ldsr<unsigned>(sb + L2_UX + 2 * o0), x1 = ldsr<unsigned>(sb + L2_UX + 2 * o1);
      const f32x2 de = den + EPS, r = {__builtin_amdgcn_rcpf(de[0]), __builtin_amdgcn_rcpf(de[1])};
      const f32x2 z = __builtin_elementwise_fma(num, r, f32x2{__uint_as_float(x0 << 16), __uint_as_float(x1 << 16)});
      const unsigned res = cvt_pk(__uint_as_float(x0 & 0xffff0000u) + fmaxf(z[0], 0.f), __uint_as_float(x1 & 0xffff0000u) + fmaxf(z[1], 0.f));
      __builtin_amdgcn_raw_buffer_store_b16((uint16_t)res, ro, o0, 0, 0);
      __builtin_amdgcn_raw_buffer_store_b16((uint16_t)(res >> 16), ro, o1, 0, 0);
    }
  };
  while (ta.np > 0) {
    const Tile tb = advance();
    issue(tb, vb);
    compute(ta, va);
    ta = advance();
    issue(ta, va);
    compute(tb, vb);
  }
  STAMPW(1, 6);
  STAMPW(1, 7);
}

extern "C" void kernel_launch(void* const* d_in, const int* in_sizes, int n_in, void* d_out, int out_size, void* d_ws, size_t ws_size,
                              hipStream_t stream) {
  const uint16_t* x = (const uint16_t*)d_in[0];
  const uint16_t* ea = (const uint16_t*)d_in[1];
  const int* src = (const int*)d_in[2];
  const int* dst = (const int*)d_in[3];
  k1_prep<<<256, 1024, 0, stream>>>(x, src, dst, (const uint16_t*)d_in[5], (const uint16_t*)d_in[8], (const uint16_t*)d_in[6], (char*)d_ws);
  k2_edge<<<256, 1024, 0, stream>>>(x, ea, (const uint16_t*)d_in[4], (const uint16_t*)d_in[7], (char*)d_ws, (uint16_t*)d_out);
}
```

```cpp
#define STAMPS
#define AMP_K0 0
#define AMP_I0 0
#define AMP_K 0
#define AMP_I 0
#define AMP_M 0
#include <hip/hip_runtime.h>
#include <stdint.h>
#pragma clang diagnostic ignored "-Wint-to-pointer-cast"

#define EXP_NOLOAD 0
#define EXP_SAMEW 0
#define EXP_OVL 3
#define LAS __attribute__((address_space(3)))
typedef unsigned u32x2 __attribute__((ext_vector_type(2)));
typedef unsigned u32x4 __attribute__((ext_vector_type(4)));
typedef float f32x2 __attribute__((ext_vector_type(2)));
typedef float f32x16 __attribute__((ext_vector_type(16)));
typedef short s16x4 __attribute__((ext_vector_type(4)));
typedef __bf16 bf16x8 __attribute__((ext_vector_type(8)));
typedef __bf16 bf16x2 __attribute__((ext_vector_type(2)));

constexpr int NN = 25000, NE = 400000;
constexpr int NB = 98;
constexpr int FB = 49;
constexpr int CH = 1568;
constexpr int OFFP = 1040;
constexpr int WSTR = 25088 * 128;
constexpr int CAPT = 61;
constexpr float NLOG2E = -1.4426950408889634f;

constexpr size_t WS_TAB = 4096;
constexpr size_t WS_REC = WS_TAB + 4 * (size_t)WSTR;
constexpr size_t WS_OFF = WS_REC + 256 * CH * 4 + 4096;
constexpr size_t WS_WC = WS_OFF + 256 * OFFP + 4096;
constexpr size_t WS_DBG = WS_WC + 4096;

#define STAMP(k, i) do { if ((threadIdx.x & 63) == 0) ((unsigned long long*)(ws + WS_DBG))[(((k) * 256 + blockIdx.x) * 8 + (i)) * 16 + (threadIdx.x >> 6)] = wall_clock64(); } while (0)
#define STAMPW STAMP
__global__ void k3_amp(char* ws) {
  const unsigned long long* st = (const unsigned long long*)(ws + WS_DBG);
  unsigned long long lo = ~0ull, hi = 0;
  for (int i = threadIdx.x; i < 256 * 16; i += 64) {
    const unsigned long long a = st[((AMP_K0 * 256 + (i >> 4)) * 8 + AMP_I0) * 16 + (i & 15)], b = st[((AMP_K * 256 + (i >> 4)) * 8 + AMP_I) * 16 + (i & 15)];
    lo = a < lo ? a : lo; hi = b > hi ? b : hi;
  }
  for (int d = 32; d; d >>= 1) { const unsigned long long a = __shfl_xor(lo, d), b = __shfl_xor(hi, d); lo = a < lo ? a : lo; hi = b > hi ? b : hi; }
  unsigned long long dt = hi - lo; if (dt > 10000) dt = 10000;
  const unsigned long long t0 = wall_clock64();
  while (wall_clock64() - t0 < dt * AMP_M) __builtin_amdgcn_s_sleep(4);
}

__device__ __forceinline__ u32x4 make_srd(const void* p, unsigned bytes) {
  const uint64_t a = (uint64_t)p;
  return u32x4{(unsigned)a, (unsigned)(a >> 32) & 0xffffu, bytes, 0x00020000u};
}
__device__ __forceinline__ void dma16(u32x4 srd, unsigned voff, unsigned lds_dst) {
  unsigned keep;
  asm volatile("s_mov_b32 %0, m0\n\ts_mov_b32 m0, %3\n\ts_nop 0\n\tbuffer_load_dwordx4 %1, %2, 0 offen lds\n\ts_mov_b32 m0, %0"
               : "=&s"(keep) : "v"(voff), "s"(srd), "s"(lds_dst) : "memory");
}
__device__ __forceinline__ void bar_lds() { asm volatile("s_waitcnt lgkmcnt(0)\n\ts_barrier" ::: "memory"); }
__device__ __forceinline__ void bar_all() { asm volatile("s_waitcnt vmcnt(0) lgkmcnt(0)\n\ts_barrier" ::: "memory"); }
template <class T> __device__ __forceinline__ T ldsr(unsigned a) { return *(const LAS T*)a; }
template <class T> __device__ __forceinline__ void ldsw(unsigned a, T v) { *(LAS T*)a = v; }
__device__ __forceinline__ unsigned lds_add_rtn(unsigned a, unsigned v) {
  return __hip_atomic_fetch_add((LAS unsigned*)a, v, __ATOMIC_RELAXED, __HIP_MEMORY_SCOPE_WORKGROUP);
}
__device__ __forceinline__ void lds_addf(unsigned a, float v) {
  __hip_atomic_fetch_add((LAS float*)a, v, __ATOMIC_RELAXED, __HIP_MEMORY_SCOPE_WORKGROUP);
}
__device__ __forceinline__ unsigned lds_poll(unsigned a) {
  const unsigned v = __builtin_amdgcn_readfirstlane(__hip_atomic_load((LAS unsigned*)a, __ATOMIC_RELAXED, __HIP_MEMORY_SCOPE_WORKGROUP));
  asm volatile("" ::: "memory");
  return v;
}
__device__ __forceinline__ void lds_wait_ge(unsigned a, unsigned n) {
  for (int i = 0; i < (1 << 20) && lds_poll(a) < n; ++i) __builtin_amdgcn_s_sleep(1);
}
__device__ __forceinline__ int lds_inc_rtn(unsigned a) {
  unsigned r;
  asm volatile("s_mov_b64 exec, 1\n\tds_add_rtn_u32 %0, %1, %2\n\ts_mov_b64 exec, -1\n\ts_waitcnt lgkmcnt(0)" : "=&v"(r) : "v"(a), "v"(1u) : "memory");
  return __builtin_amdgcn_readfirstlane(r);
}
__device__ __forceinline__ void lds_inc(unsigned a) {
  asm volatile("s_mov_b64 exec, 1\n\tds_add_u32 %0, %1\n\ts_mov_b64 exec, -1" :: "v"(a), "v"(1u) : "memory");
}
__device__ __forceinline__ u32x2 tr_read(unsigned a) {
  return __builtin_bit_cast(u32x2, __builtin_amdgcn_ds_read_tr16_b64_v4i16((LAS s16x4*)a));
}
__device__ __forceinline__ unsigned cvt_pk(float lo, float hi) {
  return __builtin_bit_cast(unsigned, bf16x2{(__bf16)lo, (__bf16)hi});
}
__device__ __forceinline__ bf16x2 as_bf2(unsigned v) { return __builtin_bit_cast(bf16x2, v); }
__device__ __forceinline__ f32x16 mfma(u32x4 a, u32x4 b, f32x16 c) {
  return __builtin_amdgcn_mfma_f32_32x32x16_bf16(__builtin_bit_cast(bf16x8, a), __builtin_bit_cast(bf16x8, b), c, 0, 0, 0);
}
__device__ __forceinline__ unsigned wave_incl_scan(unsigned v) {
  v += __builtin_amdgcn_update_dpp(0u, v, 0x111, 0xf, 0xf, true);
  v += __builtin_amdgcn_update_dpp(0u, v, 0x112, 0xf, 0xf, true);
  v += __builtin_amdgcn_update_dpp(0u, v, 0x114, 0xf, 0xf, true);
  v += __builtin_amdgcn_update_dpp(0u, v, 0x118, 0xf, 0xf, true);
  v += __builtin_amdgcn_update_dpp(0u, v, 0x142, 0xa, 0xf, true);
  v += __builtin_amdgcn_update_dpp(0u, v, 0x143, 0xc, 0xf, true);
  return v;
}

__device__ __forceinline__ void stage_wx(const void* w0, const void* w1, const void* x, int node0, unsigned ldsW, unsigned ldsX, int wv, int lane) {
  const unsigned m = ((lane >> 4) << 2) | (wv & 3);
  const unsigned voff = (4 * wv + (lane >> 4)) * 256 + 16 * ((lane & 15) ^ m);
  const u32x4 sx = make_srd(x, NN * 256), s0 = make_srd(w0, 32768), s1 = make_srd(w1, 32768);
  dma16(sx, node0 * 256 + voff, ldsX + wv * 1024);
  if (wv < 9) { if (wv < 8 || lane < 32) dma16(sx, node0 * 256 + voff + 16384, ldsX + 16384 + wv * 1024); }
  dma16(s0, voff, ldsW + wv * 1024);
  dma16(s0, voff + 16384, ldsW + 16384 + wv * 1024);
  dma16(s1, voff, ldsW + 32768 + wv * 1024);
  dma16(s1, voff + 16384, ldsW + 49152 + wv * 1024);
}
__device__ __forceinline__ void gemm_tile(unsigned ldsW, unsigned ldsX, int rt, int cb, int lane, f32x16& c0, f32x16& c1) {
  const unsigned h = lane >> 5, blk = (lane >> 4) & 1, q = (lane & 15) >> 2, p = lane & 3;
  const unsigned row = 32 * rt + (lane & 31);
  const unsigned xa = ldsX + 256 * row + 16 * (h ^ (((row & 3) << 2) | ((row >> 2) & 3)));
  const unsigned ch = 4 * cb + 2 * blk + (p >> 1);
  const unsigned wa0 = ldsW + 256 * (8 * h + q) + 16 * (ch ^ ((q << 2) | (2 * h))) + 8 * (p & 1);
  const unsigned wa1 = ldsW + 256 * (8 * h + 4 + q) + 16 * (ch ^ ((q << 2) | (2 * h + 1))) + 8 * (p & 1);
#pragma unroll
  for (int i = 0; i < 16; ++i) { c0[i] = 0.f; c1[i] = 0.f; }
#pragma unroll
  for (int s = 0; s < 8; ++s) {
    const u32x4 a = ldsr<u32x4>(xa ^ (32 * s));
    const u32x2 b00 = tr_read(wa0 + 4096 * s), b01 = tr_read(wa1 + 4096 * s);
    const u32x2 b10 = tr_read(wa0 + 4096 * s + 32768), b11 = tr_read(wa1 + 4096 * s + 32768);
    c0 = mfma(a, u32x4{b00[0], b00[1], b01[0], b01[1]}, c0);
    c1 = mfma(a, u32x4{b10[0], b10[1], b11[0], b11[1]}, c1);
  }
}

constexpr unsigned L1_W = 0, L1_X = 65536, L1_CNT = 98304, L1_OFF = L1_CNT + 2048, L1_REC = L1_OFF + 1024, L1_END = L1_REC + CH * 4;

__global__ void __launch_bounds__(1024) k1_prep(const uint16_t* __restrict__ x, const int* __restrict__ src, const int* __restrict__ dst,
                                                const uint16_t* __restrict__ wb, const uint16_t* __restrict__ wv_, const uint16_t* __restrict__ wc,
                                                char* __restrict__ ws) {
  __shared__ __attribute__((aligned(256))) char smem[L1_END];
  const unsigned sb = (unsigned)(size_t)(LAS char*)smem;
  const int tid = threadIdx.x, lane = tid & 63, wv = __builtin_amdgcn_readfirstlane(tid >> 6);
  const int g = blockIdx.x;
  const int node0 = ((g & 7) * 32 + (g >> 3)) * NB;
  STAMP(0, 0);
  const int bt = tid - 512;
  const bool bin = bt >= 0 && bt < CH / 4 && g * CH + 4 * bt < NE;
  u32x4 d4 = {0, 0, 0, 0}, s4 = {0, 0, 0, 0};
  if (bin) { d4 = *(const u32x4*)(dst + g * CH + 4 * bt); s4 = *(const u32x4*)(src + g * CH + 4 * bt); }
  stage_wx(wb, wv_, x, node0, sb + L1_W, sb + L1_X, wv, lane);
  if (tid < 512) ldsw<unsigned>(sb + L1_CNT + 4 * tid, 0u);
  if (wv == 15 && g < 4) {
    unsigned f[4];
#pragma unroll
    for (int j = 0; j < 4; ++j) {
      const int k = 8 * (lane >> 5) + 2 * j, c = 32 * g + (lane & 31);
      const float a = __uint_as_float((unsigned)wc[k * 128 + c] << 16), b = __uint_as_float((unsigned)wc[k * 128 + 128 + c] << 16);
      f[j] = cvt_pk(a * NLOG2E, b * NLOG2E);
    }
    *(u32x4*)(ws + WS_WC + g * 1024 + lane * 16) = u32x4{f[0], f[1], f[2], f[3]};
    if (lane < 32) *(unsigned*)(ws + WS_TAB + (size_t)g * WSTR + NN * 128 + 4 * lane) = 0x00007f00u;
  }
  bar_lds();
  unsigned fine[4], rank[4];
  if (bin) {
#pragma unroll
    for (int j = 0; j < 4; ++j) { fine[j] = d4[j] / FB; rank[j] = lds_add_rtn(sb + L1_CNT + 4 * fine[j], 1u); }
  }
  bar_all();
  STAMP(0, 1);
  if (wv >= 8 && wv < 15) {
    const u32x4 a = ldsr<u32x4>(sb + L1_CNT + 32 * lane), b = ldsr<u32x4>(sb + L1_CNT + 32 * lane + 16);
    const unsigned p1 = a[0], p2 = p1 + a[1], p3 = p2 + a[2], p4 = p3 + a[3], p5 = p4 + b[0], p6 = p5 + b[1], p7 = p6 + b[2], tot = p7 + b[3];
    const unsigned inc = wave_incl_scan(tot), e = inc - tot;
    const u32x4 o = {e | ((e + p1) << 16), (e + p2) | ((e + p3) << 16), (e + p4) | ((e + p5) << 16), (e + p6) | ((e + p7) << 16)};
    ldsw<u32x4>(sb + L1_OFF + 16 * lane, o);
    if (wv == 8) {
      *(u32x4*)(ws + WS_OFF + g * OFFP + 16 * lane) = o;
      if (lane == 63) *(unsigned*)(ws + WS_OFF + g * OFFP + 1024) = inc;
    }
    if (bin) {
#pragma unroll
      for (int j = 0; j < 4; ++j) {
        const unsigned pos = ldsr<uint16_t>(sb + L1_OFF + 2 * fine[j]) + rank[j];
        ldsw<unsigned>(sb + L1_REC + 4 * pos, (unsigned)(4 * bt + j) | (s4[j] << 11) | ((d4[j] - fine[j] * FB) << 26));
      }
    }
  }
  {
    const int rt = wv & 3, cb = wv >> 2;
    f32x16 cB, cV;
    gemm_tile(sb + L1_W, sb + L1_X, rt, cb, lane, cB, cV);
    STAMP(0, 2);
    char* tab = ws + WS_TAB + (size_t)cb * WSTR + (size_t)(node0 + 32 * rt + 4 * (lane >> 5)) * 128 + (lane & 31) * 4;
    const int lim = min(NB, NN - node0) - 32 * rt - 4 * (lane >> 5);
#pragma unroll
    for (int r = 0; r < 16; ++r) {
      const int rr = (r & 3) + 8 * (r >> 2);
      const unsigned lo = cvt_pk(cB[r] * NLOG2E, 0.f);
      const int u = __float_as_int(cV[r]);
      int t = __builtin_elementwise_add_sat(u, (int)(0x8000u - lo));
      t = max(t, u & (int)0x80000000);
      if (rr < lim) *(unsigned*)(tab + rr * 128) = ((unsigned)t & 0xffff0000u) | lo;
    }
  }
  bar_lds();
  if (bin) *(u32x4*)(ws + WS_REC + (size_t)(g * CH + 4 * bt) * 4) = ldsr<u32x4>(sb + L1_REC + 16 * bt);
  bar_all();
  STAMP(0, 3);
}

constexpr unsigned L2_A = 0, L2_UX = 99 * 256, L2_WC = L2_UX + 99 * 512,
                   L2_W = 0, L2_X = 65536,
                   L2_E = L2_X + NB * 256, L2_SRC = L2_E + CAPT * 1024, L2_CNT = L2_SRC + CAPT * 128, L2_HIST = L2_CNT + 400,
                   L2_SRT = L2_HIST + 256, L2_MISC = L2_SRT + 400, L2_NI = L2_MISC + 16, L2_GI = L2_NI + 400, L2_GN = L2_GI + 112,
                   L2_END = L2_GN + 208;
static_assert(L2_END <= 163840 && L2_WC + 4096 <= L2_E && L2_SRT % 16 == 0 && L2_GN % 8 == 0, "LDS map");
constexpr unsigned M_NEXT = L2_MISC, M_SLOW = L2_MISC + 8, M_INFO = L2_MISC + 12;
constexpr int NG = 25, OVL = EXP_OVL, NITEMS = 4 * NG;

struct Item { int w, t; };
__device__ __forceinline__ Item item_of(int i) {
  int w = i >= 2 * NG ? 2 : 0, r = i >= 2 * NG ? i - 2 * NG : i, m = -1;
  if (r >= NG) { r -= NG; w += 1; }
  if (r >= NG - OVL && w < 3) m = r - (NG - OVL);
  else if (r < OVL && w > 0) { m = r + OVL; w -= 1; }
  if (m >= 0) { if (m & 1) { w += 1; r = m >> 1; } else r = NG - OVL + (m >> 1); }
  return Item{w, r};
}
struct Tile { int ps, np, w, g, fl; };

#define DOT2(d, w, s, a) "v_dot2_f32_bf16 " d ", " w ", " s ", " a "\n\t"

__global__ void __launch_bounds__(1024) k2_edge(const uint16_t* __restrict__ x, const uint16_t* __restrict__ ea,
                                                const uint16_t* __restrict__ wa, const uint16_t* __restrict__ wu,
                                                char* __restrict__ ws, uint16_t* __restrict__ out) {
  __shared__ __attribute__((aligned(256))) char smem[L2_END];
  const unsigned sb = (unsigned)(size_t)(LAS char*)smem;
  const int tid = threadIdx.x, lane = tid & 63, wv = __builtin_amdgcn_readfirstlane(tid >> 6);
  const int bk = (blockIdx.x & 7) * 32 + (blockIdx.x >> 3), node0 = bk * NB;
  const int h = lane >> 5, c = lane & 31;
  STAMP(1, 0);
  const int seg = tid >> 2, sj = tid & 3;
  const u32x2 o2 = *(const u32x2*)(ws + WS_OFF + seg * OFFP + 4 * bk);
  if (tid < (L2_NI - L2_CNT) / 4) {
    const unsigned a = L2_CNT + 4 * tid;
    ldsw<unsigned>(sb + a, a == M_NEXT ? 16u : (a >= L2_SRT && a < L2_MISC) ? (unsigned)NB : 0u);
  }
  if (tid < CAPT * 8) {
    const unsigned dv = NN * 128 + 128 - 128 * (tid & 1);
    ldsw<u32x4>(sb + L2_SRC + 16 * tid, u32x4{dv, dv, dv, dv});
    ldsw<u32x4>(sb + L2_E + (tid >> 3) * 1024 + (tid & 7) * 16, u32x4{0, 0, 0, 0});
  }
  bar_lds();
  const int s0 = o2[0] & 0xffff, s1 = o2[0] >> 16, len = (o2[1] & 0xffff) - s0;
  const unsigned* recs = (const unsigned*)(ws + WS_REC) + seg * CH + s0;
  const bool big = __any(len > 16);
  u32x4 ra = {0, 0, 0, 0}, rb = {0, 0, 0, 0};
  if (4 * sj < len) ra = *(const u32x4*)(recs + 4 * sj);
  if (big) { if (16 + 4 * sj < len) rb = *(const u32x4*)(recs + 16 + 4 * sj); if (len > 32) ldsw<unsigned>(sb + M_SLOW, 1u); }
  STAMP(1, 1);
  unsigned na[4], ka[4], nb_[4], kb[4];
#pragma unroll
  for (int j = 0; j < 4; ++j) {
    const int i = 4 * sj + j;
    na[j] = (ra[j] >> 26) + (s0 + i >= s1 ? FB : 0); ka[j] = 0;
    if (i < len) ka[j] = lds_add_rtn(sb + L2_CNT + 4 * na[j], 1u);
  }
  if (big) {
#pragma unroll
    for (int j = 0; j < 4; ++j) {
      const int i = 16 + 4 * sj + j;
      nb_[j] = (rb[j] >> 26) + (s0 + i >= s1 ? FB : 0); kb[j] = 0;
      if (i < len) kb[j] = lds_add_rtn(sb + L2_CNT + 4 * nb_[j], 1u);
    }
  }
  u32x4 wcv = {0, 0, 0, 0};
  if (wv >= 12) wcv = *(const u32x4*)(ws + WS_WC + (tid - 768) * 16);
  stage_wx(wa, wu, x, node0, sb + L2_W, sb + L2_X, wv, lane);
  bar_lds();
  STAMP(1, 2);
  if (wv == 0) {
    unsigned k0 = 0, k1 = 0, b0 = 0, b1 = 0, r0 = 0, r1 = 0;
    if (lane < NB / 2) {
      const u32x2 cn = ldsr<u32x2>(sb + L2_CNT + 8 * lane);
      k0 = (cn[0] + 1) >> 1; k1 = (cn[1] + 1) >> 1;
      b0 = 63 - min(k0, 63u); b1 = 63 - min(k1, 63u);
      r0 = lds_add_rtn(sb + L2_HIST + 4 * b0, 1u); r1 = lds_add_rtn(sb + L2_HIST + 4 * b1, 1u);
    }
    const unsigned hv = __hip_atomic_load((LAS unsigned*)(sb + L2_HIST + 4 * lane), __ATOMIC_RELAXED, __HIP_MEMORY_SCOPE_WORKGROUP);
    const unsigned hb = wave_incl_scan(hv) - hv;
    const unsigned p0 = __builtin_amdgcn_ds_bpermute(4 * b0, hb) + r0, p1 = __builtin_amdgcn_ds_bpermute(4 * b1, hb) + r1;
    if (lane < NB / 2) { ldsw<unsigned>(sb + L2_SRT + 4 * p0, (2 * lane) | (k0 << 8)); ldsw<unsigned>(sb + L2_SRT + 4 * p1, (2 * lane + 1) | (k1 << 8)); }
    asm volatile("" ::: "memory");
    u32x4 q = {0, 0, 0, 0};
    unsigned gl = 0;
    if (lane < NG) { q = ldsr<u32x4>(sb + L2_SRT + 16 * lane); gl = max(max(max(q[0], q[1]), max(q[2], q[3])) >> 8, 1u); }
    const unsigned inc = wave_incl_scan(gl), gb = inc - gl;
    if (lane < NG) {
      ldsw<unsigned>(sb + L2_GI + 4 * lane, gb | (gl << 16));
      ldsw<u32x2>(sb + L2_GN + 8 * lane, u32x2{((q[0] & 255) << 8) | ((q[1] & 255) << 24), ((q[2] & 255) << 8) | ((q[3] & 255) << 24)});
#pragma unroll
      for (int j = 0; j < 4; ++j) ldsw<unsigned>(sb + L2_NI + 4 * (q[j] & 255), gb * 8 + (j >> 1) * 4 + (j & 1));
    }
    if (lane == 63) { ldsw<unsigned>(sb + M_INFO, inc); if (inc > CAPT * 4) ldsw<unsigned>(sb + M_SLOW, 1u); }
  }
  bar_lds();
  const u32x2 inf = ldsr<u32x2>(sb + M_SLOW);
  const bool slow = __builtin_amdgcn_readfirstlane(inf[0]) != 0;
  const int nte = slow ? 0 : (__builtin_amdgcn_readfirstlane(inf[1]) + 3) >> 2;
  auto place = [&](unsigned rec, unsigned node, unsigned k) __attribute__((always_inline)) {
    const unsigned L = ldsr<unsigned>(sb + L2_NI + 4 * node) + (k >> 1) * 8 + (k & 1) * 2;
    ldsw<unsigned>(sb + L2_SRC + 4 * L, ((rec >> 11) & 0x7fff) * 128 + 128 - 32 * (L & 4));
    ldsw<unsigned>(sb + L2_E + (L >> 5) * 1024 + (L & 31) * 4, (seg * CH + (rec & 0x7ff)) * 32);
  };
  if (!slow) {
#pragma unroll
    for (int j = 0; j < 4; ++j) if (4 * sj + j < len) place(ra[j], na[j], ka[j]);
    if (big) {
#pragma unroll
      for (int j = 0; j < 4; ++j) if (16 + 4 * sj + j < len) place(rb[j], nb_[j], kb[j]);
    }
  }
  bar_all();
  STAMP(1, 3);
  {
    const u32x4 se = make_srd(ea, NE * 32);
    unsigned eo[4];
#pragma unroll
    for (int j = 0; j < 4; ++j) { eo[j] = 0; if (wv + 16 * j < nte) eo[j] = ldsr<unsigned>(sb + L2_E + (wv + 16 * j) * 1024 + 4 * (lane >> 1)) + 16 * (lane & 1); }
#pragma unroll
    for (int j = 0; j < 4; ++j) if (wv + 16 * j < nte) dma16(se, eo[j], sb + L2_E + (wv + 16 * j) * 1024);
  }
  int s_ps = 0, s_rem = 0, s_w = 0, s_g = 0;
  auto open_item = [&](int idx) __attribute__((always_inline)) {
    const Item it = item_of(min(idx, NITEMS - 1));
    const unsigned gi = __builtin_amdgcn_readfirstlane(ldsr<unsigned>(sb + L2_GI + 4 * it.t));
    s_w = it.w; s_g = it.t; s_ps = gi & 0xffff; s_rem = idx < NITEMS ? gi >> 16 : 0;
  };
  auto take = [&](int fl) __attribute__((always_inline)) {
    const int np = min(s_rem, 4);
    const Tile t = {s_ps, np, s_w, s_g, fl | (s_rem == np ? 2 : 0)};
    s_ps += np; s_rem -= np;
    return t;
  };
  auto advance = [&]() __attribute__((always_inline)) {
    int fl = 0;
    if (s_rem == 0) { open_item(lds_inc_rtn(sb + M_NEXT)); fl = 1; }
    return take(fl);
  };
  auto issue = [&](const Tile& t, unsigned (&v)[16]) __attribute__((always_inline)) {
    const unsigned la = sb + L2_SRC + t.ps * 32 + h * 16;
    const int so = EXP_SAMEW ? 0 : t.w * WSTR;
#pragma unroll
    for (int u = 0; u < 4; ++u) {
      const auto rs = __builtin_amdgcn_make_buffer_rsrc(ws + WS_TAB - 128, (short)4, (u < t.np && !EXP_NOLOAD) ? 4 * WSTR + 128 : 0, 1 << 23);
      const u32x4 q = ldsr<u32x4>(la + 32 * u);
#pragma unroll
      for (int z = 0; z < 4; ++z) v[4 * u + z] = __builtin_amdgcn_raw_buffer_load_b32(rs, q[z], so, 0);
    }
  };
  unsigned va[16], vb[16];
  open_item(slow ? NITEMS : wv);
  if (slow) s_ps = 0;
  Tile ta = take(1);
  issue(ta, va);
  {
    const int rt = wv & 3, ow = wv >> 2;
    f32x16 cA, cU;
    gemm_tile(sb + L2_W, sb + L2_X, rt, ow, lane, cA, cU);
    const unsigned q = (lane & 15) >> 2, p = lane & 3, ch = 4 * ow + 2 * ((lane >> 4) & 1) + (p >> 1);
    unsigned ux[16];
#pragma unroll
    for (int g = 0; g < 4; ++g) {
      const unsigned row = 32 * rt + 8 * g + 4 * h + q;
      const u32x2 xx = tr_read(sb + L2_X + 256 * row + 16 * (ch ^ ((q << 2) | ((2 * g + h) & 3))) + 8 * (p & 1));
      const unsigned u01 = cvt_pk(cU[4 * g], cU[4 * g + 1]), u23 = cvt_pk(cU[4 * g + 2], cU[4 * g + 3]);
      ux[4 * g] = __builtin_amdgcn_perm(xx[0], u01, 0x05040100); ux[4 * g + 1] = __builtin_amdgcn_perm(xx[0], u01, 0x07060302);
      ux[4 * g + 2] = __builtin_amdgcn_perm(xx[1], u23, 0x05040100); ux[4 * g + 3] = __builtin_amdgcn_perm(xx[1], u23, 0x07060302);
    }
    bar_lds();
    STAMP(1, 4);
    const unsigned ab = (32 * rt + 4 * h) * 256 + (32 * ow + c) * 2;
    auto put_a = [&](int r) __attribute__((always_inline)) {
      const int rr = (r & 3) + 8 * (r >> 2);
      ldsw<uint16_t>(sb + L2_A + ab + rr * 256, (uint16_t)cvt_pk(cA[r] * NLOG2E, 0.f));
      ldsw<unsigned>(sb + L2_UX + 2 * ab + rr * 512, ux[r]);
    };
    if (rt < 3 || h == 0) { put_a(0); put_a(1); }
    if (rt < 3) {
#pragma unroll
      for (int r = 2; r < 16; ++r) put_a(r);
    }
    if (wv >= 12) ldsw<u32x4>(sb + L2_WC + (tid - 768) * 16, wcv);
  }
  asm volatile("s_waitcnt vmcnt(16) lgkmcnt(0)\n\ts_barrier" ::: "memory");
  STAMP(1, 5);

  const auto ro = __builtin_amdgcn_make_buffer_rsrc(out + (size_t)node0 * 128, (short)0, min(NB, NN - node0) * 256, 0x00020000);
  constexpr float EPS = 1e-6f * 0x1p30f;
  if (slow) {
    for (int w = 0; w < 4; ++w) {
      for (int i = tid; i < NB * 64; i += 1024) ldsw<unsigned>(sb + L2_E + 4 * i, 0u);
      bar_lds();
      const u32x4 w0 = ldsr<u32x4>(sb + L2_WC + w * 1024 + c * 16), w1 = ldsr<u32x4>(sb + L2_WC + w * 1024 + 512 + c * 16);
      for (int sg = wv; sg < 256; sg += 16) {
        const u32x2 oo = *(const u32x2*)(ws + WS_OFF + sg * OFFP + 4 * bk);
        const int b0 = oo[0] & 0xffff, b1 = oo[0] >> 16, b2 = oo[1] & 0xffff;
        for (int i = b0 + h; i < b2; i += 2) {
          const unsigned rec = ((const unsigned*)(ws + WS_REC))[sg * CH + i];
          const unsigned node = (rec >> 26) + (i >= b1 ? FB : 0), sn = (rec >> 11) & 0x7fff, eid = sg * CH + (rec & 0x7ff);
          const u32x4 e0 = *(const u32x4*)(ea + eid * 16), e1 = *(const u32x4*)(ea + eid * 16 + 8);
          const unsigned wd = *(const unsigned*)(ws + WS_TAB + (size_t)w * WSTR + sn * 128 + 4 * c);
          float gs = __uint_as_float((unsigned)ldsr<uint16_t>(sb + L2_A + node * 256 + (32 * w + c) * 2) << 16) + __uint_as_float(wd << 16);
#pragma unroll
          for (int j = 0; j < 4; ++j) {
            gs = __builtin_amdgcn_fdot2_f32_bf16(as_bf2(e0[j]), as_bf2(w0[j]), gs, false);
            gs = __builtin_amdgcn_fdot2_f32_bf16(as_bf2(e1[j]), as_bf2(w1[j]), gs, false);
          }
          const float eta = __builtin_amdgcn_rcpf(fmaf(__builtin_amdgcn_exp2f(gs), 0x1p-30f, 0x1p-30f));
          lds_addf(sb + L2_E + node * 256 + 4 * c, eta * __uint_as_float(wd)); lds_addf(sb + L2_E + node * 256 + 128 + 4 * c, eta);
        }
      }
      bar_lds();
      for (int n = 2 * wv + h; n < NB; n += 32) {
        const unsigned o = n * 256 + (32 * w + c) * 2, ux = ldsr<unsigned>(sb + L2_UX + 2 * o);
        const float z = fmaf(ldsr<float>(sb + L2_E + n * 256 + 4 * c), __builtin_amdgcn_rcpf(ldsr<float>(sb + L2_E + n * 256 + 128 + 4 * c) + EPS), __uint_as_float(ux << 16));
        __builtin_amdgcn_raw_buffer_store_b16((uint16_t)cvt_pk(__uint_as_float(ux & 0xffff0000u) + fmaxf(z, 0.f), 0.f), ro, o, 0, 0);
      }
      bar_lds();
    }
    return;
  }

  const unsigned one_lo = 0x00003f80u;
  unsigned o0 = 0, o1 = 0;
  f32x2 A2 = {0.f, 0.f}, num = {0.f, 0.f}, den = {0.f, 0.f};
  u32x4 wf = {0, 0, 0, 0};
  auto compute = [&](const Tile& t, unsigned (&v)[16]) __attribute__((always_inline)) {
    if (t.np == 0) return;
    const u32x4 ef = ldsr<u32x4>(sb + L2_E + t.ps * 256 + c * 32 + h * 16);
    if (t.fl & 1) {
      const unsigned nd = ldsr<unsigned>(sb + L2_GN + 8 * t.g + 4 * h), fo = (32 * t.w + c) * 2;
      wf = ldsr<u32x4>(sb + L2_WC + t.w * 1024 + lane * 16);
      o0 = (nd & 0xffff) + fo; o1 = (nd >> 16) + fo;
      A2 = f32x2{__uint_as_float((unsigned)ldsr<uint16_t>(sb + L2_A + o0) << 16), __uint_as_float((unsigned)ldsr<uint16_t>(sb + L2_A + o1) << 16)} - 60.f;
      num = f32x2{0.f, 0.f}; den = f32x2{0.f, 0.f};
    }
    float d[16];
#pragma unroll
    for (int u = 0; u < 4; ++u) {
      if (u == 0 || u < t.np)
        asm(DOT2("%0", "%4", "%8", "%9") DOT2("%1", "%5", "%8", "%10") DOT2("%2", "%6", "%8", "%9") DOT2("%3", "%7", "%8", "%10")
            : "=&v"(d[4 * u]), "=&v"(d[4 * u + 1]), "=&v"(d[4 * u + 2]), "=&v"(d[4 * u + 3])
            : "v"(v[4 * u]), "v"(v[4 * u + 1]), "v"(v[4 * u + 2]), "v"(v[4 * u + 3]), "s"(one_lo), "v"(A2[0]), "v"(A2[1]));
      else
        asm("" : "=v"(d[4 * u]), "=v"(d[4 * u + 1]), "=v"(d[4 * u + 2]), "=v"(d[4 * u + 3]));
    }
    f32x16 g;
#pragma unroll
    for (int r = 0; r < 16; ++r) g[r] = d[r];
    asm volatile("s_nop 2" : "+v"(g));
    g = mfma(ef, wf, g);
#pragma unroll
    for (int u = 0; u < 4; ++u) {
      if (u == 0 || u < t.np) {
        const f32x2 t0 = {__builtin_amdgcn_fmed3f(__builtin_amdgcn_exp2f(g[4 * u]), 0.f, 1.f), __builtin_amdgcn_fmed3f(__builtin_amdgcn_exp2f(g[4 * u + 1]), 0.f, 1.f)};
        const f32x2 t1 = {__builtin_amdgcn_fmed3f(__builtin_amdgcn_exp2f(g[4 * u + 2]), 0.f, 1.f), __builtin_amdgcn_fmed3f(__builtin_amdgcn_exp2f(g[4 * u + 3]), 0.f, 1.f)};
        const f32x2 a0 = __builtin_elementwise_fma(t0, f32x2{0x1p30f, 0x1p30f}, f32x2{0x1p-30f, 0x1p-30f});
        const f32x2 a1 = __builtin_elementwise_fma(t1, f32x2{0x1p30f, 0x1p30f}, f32x2{0x1p-30f, 0x1p-30f});
        const f32x2 v0 = {__uint_as_float(v[4 * u]), __uint_as_float(v[4 * u + 1])}, v1 = {__uint_as_float(v[4 * u + 2]), __uint_as_float(v[4 * u + 3])};
        const f32x2 p = a0 * a1, r = {__builtin_amdgcn_rcpf(p[0]), __builtin_amdgcn_rcpf(p[1])};
        num = __builtin_elementwise_fma(__builtin_elementwise_fma(v1, a0, v0 * a1), r, num);
        den = __builtin_elementwise_fma(a0 + a1, r, den);
      }
    }
    if (t.fl & 2) {
      const unsigned x0 = ldsr<unsigned>(sb + L2_UX + 2 * o0), x1 = ldsr<unsigned>(sb + L2_UX + 2 * o1);
      const f32x2 de = den + EPS, r = {__builtin_amdgcn_rcpf(de[0]), __builtin_amdgcn_rcpf(de[1])};
      const f32x2 z = __builtin_elementwise_fma(num, r, f32x2{__uint_as_float(x0 << 16), __uint_as_float(x1 << 16)});
      const unsigned res = cvt_pk(__uint_as_float(x0 & 0xffff0000u) + fmaxf(z[0], 0.f), __uint_as_float(x1 & 0xffff0000u) + fmaxf(z[1], 0.f));
      __builtin_amdgcn_raw_buffer_store_b16((uint16_t)res, ro, o0, 0, 0);
      __builtin_amdgcn_raw_buffer_store_b16((uint16_t)(res >> 16), ro, o1, 0, 0);
    }
  };
  while (ta.np > 0) {
    const Tile tb = advance();
    issue(tb, vb);
    compute(ta, va);
    ta = advance();
    issue(ta, va);
    compute(tb, vb);
  }
  STAMPW(1, 6);
  asm volatile("s_waitcnt vmcnt(0)" ::: "memory");
  STAMPW(1, 7);
}

extern "C" void kernel_launch(void* const* d_in, const int* in_sizes, int n_in, void* d_out, int out_size, void* d_ws, size_t ws_size,
                              hipStream_t stream) {
  const uint16_t* x = (const uint16_t*)d_in[0];
  const uint16_t* ea = (const uint16_t*)d_in[1];
  const int* src = (const int*)d_in[2];
  const int* dst = (const int*)d_in[3];
  k1_prep<<<256, 1024, 0, stream>>>(x, src, dst, (const uint16_t*)d_in[5], (const uint16_t*)d_in[8], (const uint16_t*)d_in[6], (char*)d_ws);
  k2_edge<<<256, 1024, 0, stream>>>(x, ea, (const uint16_t*)d_in[4], (const uint16_t*)d_in[7], (char*)d_ws, (uint16_t*)d_out);
  k3_amp<<<1, 64, 0, stream>>>((char*)d_ws);
}
```

```cpp
#define STAMPS
#define AMP_K0 0
#define AMP_I0 0
#define AMP_K 1
#define AMP_I 7
#define AMP_M 20
#define AMP_MODE 1
#include <hip/hip_runtime.h>
#include <stdint.h>
#pragma clang diagnostic ignored "-Wint-to-pointer-cast"

#define EXP_NOLOAD 0
#define EXP_SAMEW 0
#define EXP_OVL 3
#define LAS __attribute__((address_space(3)))
typedef unsigned u32x2 __attribute__((ext_vector_type(2)));
typedef unsigned u32x4 __attribute__((ext_vector_type(4)));
typedef float f32x2 __attribute__((ext_vector_type(2)));
typedef float f32x16 __attribute__((ext_vector_type(16)));
typedef short s16x4 __attribute__((ext_vector_type(4)));
typedef __bf16 bf16x8 __attribute__((ext_vector_type(8)));
typedef __bf16 bf16x2 __attribute__((ext_vector_type(2)));

constexpr int NN = 25000, NE = 400000;
constexpr int NB = 98;
constexpr int FB = 49;
constexpr int CH = 1568;
constexpr int OFFP = 1040;
constexpr int WSTR = 25088 * 128;
constexpr int CAPT = 61;
constexpr float NLOG2E = -1.4426950408889634f;

constexpr size_t WS_TAB = 4096;
constexpr size_t WS_REC = WS_TAB + 4 * (size_t)WSTR;
constexpr size_t WS_OFF = WS_REC + 256 * CH * 4 + 4096;
constexpr size_t WS_WC = WS_OFF + 256 * OFFP + 4096;
constexpr size_t WS_DBG = WS_WC + 4096;

#define STAMP(k, i) do { if ((threadIdx.x & 63) == 0) ((unsigned long long*)(ws + WS_DBG))[(((k) * 256 + blockIdx.x) * 8 + (i)) * 16 + (threadIdx.x >> 6)] = wall_clock64(); } while (0)
#define STAMPW STAMP
__global__ void k0_stamp(char* ws) { ((unsigned long long*)(ws + WS_DBG))[2 * 256 * 8 * 16] = wall_clock64(); }
__global__ void k3_amp(char* ws) {
  const unsigned long long tk3 = wall_clock64();
  const unsigned long long* st = (const unsigned long long*)(ws + WS_DBG);
  unsigned long long lo = ~0ull, hi = 0;
  for (int i = threadIdx.x; i < 256 * 16; i += 64) {
    const unsigned long long a = st[((AMP_K0 * 256 + (i >> 4)) * 8 + AMP_I0) * 16 + (i & 15)], b = st[((AMP_K * 256 + (i >> 4)) * 8 + AMP_I) * 16 + (i & 15)];
    lo = a < lo ? a : lo; hi = b > hi ? b : hi;
  }
  for (int d = 32; d; d >>= 1) { const unsigned long long a = __shfl_xor(lo, d), b = __shfl_xor(hi, d); lo = a < lo ? a : lo; hi = b > hi ? b : hi; }
  unsigned long long dt = hi - lo;
  dt = dt == 0x123456789ull;
  if (dt > 10000) dt = 10000;
  const unsigned long long t0 = wall_clock64();
  while (wall_clock64() - t0 < dt * AMP_M) __builtin_amdgcn_s_sleep(4);
}

__device__ __forceinline__ u32x4 make_srd(const void* p, unsigned bytes) {
  const uint64_t a = (uint64_t)p;
  return u32x4{(unsigned)a, (unsigned)(a >> 32) & 0xffffu, bytes, 0x00020000u};
}
__device__ __forceinline__ void dma16(u32x4 srd, unsigned voff, unsigned lds_dst) {
  unsigned keep;
  asm volatile("s_mov_b32 %0, m0\n\ts_mov_b32 m0, %3\n\ts_nop 0\n\tbuffer_load_dwordx4 %1, %2, 0 offen lds\n\ts_mov_b32 m0, %0"
               : "=&s"(keep) : "v"(voff), "s"(srd), "s"(lds_dst) : "memory");
}
__device__ __forceinline__ void bar_lds() { asm volatile("s_waitcnt lgkmcnt(0)\n\ts_barrier" ::: "memory"); }
__device__ __forceinline__ void bar_all() { asm volatile("s_waitcnt vmcnt(0) lgkmcnt(0)\n\ts_barrier" ::: "memory"); }
template <class T> __device__ __forceinline__ T ldsr(unsigned a) { return *(const LAS T*)a; }
template <class T> __device__ __forceinline__ void ldsw(unsigned a, T v) { *(LAS T*)a = v; }
__device__ __forceinline__ unsigned lds_add_rtn(unsigned a, unsigned v) {
  return __hip_atomic_fetch_add((LAS unsigned*)a, v, __ATOMIC_RELAXED, __HIP_MEMORY_SCOPE_WORKGROUP);
}
__device__ __forceinline__ void lds_addf(unsigned a, float v) {
  __hip_atomic_fetch_add((LAS float*)a, v, __ATOMIC_RELAXED, __HIP_MEMORY_SCOPE_WORKGROUP);
}
__device__ __forceinline__ unsigned lds_poll(unsigned a) {
  const unsigned v = __builtin_amdgcn_readfirstlane(__hip_atomic_load((LAS unsigned*)a, __ATOMIC_RELAXED, __HIP_MEMORY_SCOPE_WORKGROUP));
  asm volatile("" ::: "memory");
  return v;
}
__device__ __forceinline__ void lds_wait_ge(unsigned a, unsigned n) {
  for (int i = 0; i < (1 << 20) && lds_poll(a) < n; ++i) __builtin_amdgcn_s_sleep(1);
}
__device__ __forceinline__ int lds_inc_rtn(unsigned a) {
  unsigned r;
  asm volatile("s_mov_b64 exec, 1\n\tds_add_rtn_u32 %0, %1, %2\n\ts_mov_b64 exec, -1\n\ts_waitcnt lgkmcnt(0)" : "=&v"(r) : "v"(a), "v"(1u) : "memory");
  return __builtin_amdgcn_readfirstlane(r);
}
__device__ __forceinline__ void lds_inc(unsigned a) {
  asm volatile("s_mov_b64 exec, 1\n\tds_add_u32 %0, %1\n\ts_mov_b64 exec, -1" :: "v"(a), "v"(1u) : "memory");
}
__device__ __forceinline__ u32x2 tr_read(unsigned a) {
  return __builtin_bit_cast(u32x2, __builtin_amdgcn_ds_read_tr16_b64_v4i16((LAS s16x4*)a));
}
__device__ __forceinline__ unsigned cvt_pk(float lo, float hi) {
  return __builtin_bit_cast(unsigned, bf16x2{(__bf16)lo, (__bf16)hi});
}
__device__ __forceinline__ bf16x2 as_bf2(unsigned v) { return __builtin_bit_cast(bf16x2, v); }
__device__ __forceinline__ f32x16 mfma(u32x4 a, u32x4 b, f32x16 c) {
  return __builtin_amdgcn_mfma_f32_32x32x16_bf16(__builtin_bit_cast(bf16x8, a), __builtin_bit_cast(bf16x8, b), c, 0, 0, 0);
}
__device__ __forceinline__ unsigned wave_incl_scan(unsigned v) {
  v += __builtin_amdgcn_update_dpp(0u, v, 0x111, 0xf, 0xf, true);
  v += __builtin_amdgcn_update_dpp(0u, v, 0x112, 0xf, 0xf, true);
  v += __builtin_amdgcn_update_dpp(0u, v, 0x114, 0xf, 0xf, true);
  v += __builtin_amdgcn_update_dpp(0u, v, 0x118, 0xf, 0xf, true);
  v += __builtin_amdgcn_update_dpp(0u, v, 0x142, 0xa, 0xf, true);
  v += __builtin_amdgcn_update_dpp(0u, v, 0x143, 0xc, 0xf, true);
  return v;
}

__device__ __forceinline__ void stage_wx(const void* w0, const void* w1, const void* x, int node0, unsigned ldsW, unsigned ldsX, int wv, int lane) {
  const unsigned m = ((lane >> 4) << 2) | (wv & 3);
  const unsigned voff = (4 * wv + (lane >> 4)) * 256 + 16 * ((lane & 15) ^ m);
  const u32x4 sx = make_srd(x, NN * 256), s0 = make_srd(w0, 32768), s1 = make_srd(w1, 32768);
  dma16(sx, node0 * 256 + voff, ldsX + wv * 1024);
  if (wv < 9) { if (wv < 8 || lane < 32) dma16(sx, node0 * 256 + voff + 16384, ldsX + 16384 + wv * 1024); }
  dma16(s0, voff, ldsW + wv * 1024);
  dma16(s0, voff + 16384, ldsW + 16384 + wv * 1024);
  dma16(s1, voff, ldsW + 32768 + wv * 1024);
  dma16(s1, voff + 16384, ldsW + 49152 + wv * 1024);
}
__device__ __forceinline__ void gemm_tile(unsigned ldsW, unsigned ldsX, int rt, int cb, int lane, f32x16& c0, f32x16& c1) {
  const unsigned h = lane >> 5, blk = (lane >> 4) & 1, q = (lane & 15) >> 2, p = lane & 3;
  const unsigned row = 32 * rt + (lane & 31);
  const unsigned xa = ldsX + 256 * row + 16 * (h ^ (((row & 3) << 2) | ((row >> 2) & 3)));
  const unsigned ch = 4 * cb + 2 * blk + (p >> 1);
  const unsigned wa0 = ldsW + 256 * (8 * h + q) + 16 * (ch ^ ((q << 2) | (2 * h))) + 8 * (p & 1);
  const unsigned wa1 = ldsW + 256 * (8 * h + 4 + q) + 16 * (ch ^ ((q << 2) | (2 * h + 1))) + 8 * (p & 1);
#pragma unroll
  for (int i = 0; i < 16; ++i) { c0[i] = 0.f; c1[i] = 0.f; }
#pragma unroll
  for (int s = 0; s < 8; ++s) {
    const u32x4 a = ldsr<u32x4>(xa ^ (32 * s));
    const u32x2 b00 = tr_read(wa0 + 4096 * s), b01 = tr_read(wa1 + 4096 * s);
    const u32x2 b10 = tr_read(wa0 + 4096 * s + 32768), b11 = tr_read(wa1 + 4096 * s + 32768);
    c0 = mfma(a, u32x4{b00[0], b00[1], b01[0], b01[1]}, c0);
    c1 = mfma(a, u32x4{b10[0], b10[1], b11[0], b11[1]}, c1);
  }
}

constexpr unsigned L1_W = 0, L1_X = 65536, L1_CNT = 98304, L1_OFF = L1_CNT + 2048, L1_REC = L1_OFF + 1024, L1_END = L1_REC + CH * 4;

__global__ void __launch_bounds__(1024) k1_prep(const uint16_t* __restrict__ x, const int* __restrict__ src, const int* __restrict__ dst,
                                                const uint16_t* __restrict__ wb, const uint16_t* __restrict__ wv_, const uint16_t* __restrict__ wc,
                                                char* __restrict__ ws) {
  __shared__ __attribute__((aligned(256))) char smem[L1_END];
  const unsigned sb = (unsigned)(size_t)(LAS char*)smem;
  const int tid = threadIdx.x, lane = tid & 63, wv = __builtin_amdgcn_readfirstlane(tid >> 6);
  const int g = blockIdx.x;
  const int node0 = ((g & 7) * 32 + (g >> 3)) * NB;
  STAMP(0, 0);
  const int bt = tid - 512;
  const bool bin = bt >= 0 && bt < CH / 4 && g * CH + 4 * bt < NE;
  u32x4 d4 = {0, 0, 0, 0}, s4 = {0, 0, 0, 0};
  if (bin) { d4 = *(const u32x4*)(dst + g * CH + 4 * bt); s4 = *(const u32x4*)(src + g * CH + 4 * bt); }
  stage_wx(wb, wv_, x, node0, sb + L1_W, sb + L1_X, wv, lane);
  if (tid < 512) ldsw<unsigned>(sb + L1_CNT + 4 * tid, 0u);
  if (wv == 15 && g < 4) {
    unsigned f[4];
#pragma unroll
    for (int j = 0; j < 4; ++j) {
      const int k = 8 * (lane >> 5) + 2 * j, c = 32 * g + (lane & 31);
      const float a = __uint_as_float((unsigned)wc[k * 128 + c] << 16), b = __uint_as_float((unsigned)wc[k * 128 + 128 + c] << 16);
      f[j] = cvt_pk(a * NLOG2E, b * NLOG2E);
    }
    *(u32x4*)(ws + WS_WC + g * 1024 + lane * 16) = u32x4{f[0], f[1], f[2], f[3]};
    if (lane < 32) *(unsigned*)(ws + WS_TAB + (size_t)g * WSTR + NN * 128 + 4 * lane) = 0x00007f00u;
  }
  bar_lds();
  unsigned fine[4], rank[4];
  if (bin) {
#pragma unroll
    for (int j = 0; j < 4; ++j) { fine[j] = d4[j] / FB; rank[j] = lds_add_rtn(sb + L1_CNT + 4 * fine[j], 1u); }
  }
  bar_all();
  STAMP(0, 1);
  if (wv >= 8 && wv < 15) {
    const u32x4 a = ldsr<u32x4>(sb + L1_CNT + 32 * lane), b = ldsr<u32x4>(sb + L1_CNT + 32 * lane + 16);
    const unsigned p1 = a[0], p2 = p1 + a[1], p3 = p2 + a[2], p4 = p3 + a[3], p5 = p4 + b[0], p6 = p5 + b[1], p7 = p6 + b[2], tot = p7 + b[3];
    const unsigned inc = wave_incl_scan(tot), e = inc - tot;
    const u32x4 o = {e | ((e + p1) << 16), (e + p2) | ((e + p3) << 16), (e + p4) | ((e + p5) << 16), (e + p6) | ((e + p7) << 16)};
    ldsw<u32x4>(sb + L1_OFF + 16 * lane, o);
    if (wv == 8) {
      *(u32x4*)(ws + WS_OFF + g * OFFP + 16 * lane) = o;
      if (lane == 63) *(unsigned*)(ws + WS_OFF + g * OFFP + 1024) = inc;
    }
    if (bin) {
#pragma unroll
      for (int j = 0; j < 4; ++j) {
        const unsigned pos = ldsr<uint16_t>(sb + L1_OFF + 2 * fine[j]) + rank[j];
        ldsw<unsigned>(sb + L1_REC + 4 * pos, (unsigned)(4 * bt + j) | (s4[j] << 11) | ((d4[j] - fine[j] * FB) << 26));
      }
    }
  }
  {
    const int rt = wv & 3, cb = wv >> 2;
    f32x16 cB, cV;
    gemm_tile(sb + L1_W, sb + L1_X, rt, cb, lane, cB, cV);
    STAMP(0, 2);
    char* tab = ws + WS_TAB + (size_t)cb * WSTR + (size_t)(node0 + 32 * rt + 4 * (lane >> 5)) * 128 + (lane & 31) * 4;
    const int lim = min(NB, NN - node0) - 32 * rt - 4 * (lane >> 5);
#pragma unroll
    for (int r = 0; r < 16; ++r) {
      const int rr = (r & 3) + 8 * (r >> 2);
      const unsigned lo = cvt_pk(cB[r] * NLOG2E, 0.f);
      const int u = __float_as_int(cV[r]);
      int t = __builtin_elementwise_add_sat(u, (int)(0x8000u - lo));
      t = max(t, u & (int)0x80000000);
      if (rr < lim) *(unsigned*)(tab + rr * 128) = ((unsigned)t & 0xffff0000u) | lo;
    }
  }
  bar_lds();
  if (bin) *(u32x4*)(ws + WS_REC + (size_t)(g * CH + 4 * bt) * 4) = ldsr<u32x4>(sb + L1_REC + 16 * bt);
  bar_all();
  STAMP(0, 3);
}

constexpr unsigned L2_A = 0, L2_UX = 99 * 256, L2_WC = L2_UX + 99 * 512,
                   L2_W = 0, L2_X = 65536,
                   L2_E = L2_X + NB * 256, L2_SRC = L2_E + CAPT * 1024, L2_CNT = L2_SRC + CAPT * 128, L2_HIST = L2_CNT + 400,
                   L2_SRT = L2_HIST + 256, L2_MISC = L2_SRT + 400, L2_NI = L2_MISC + 16, L2_GI = L2_NI + 400, L2_GN = L2_GI + 112,
                   L2_END = L2_GN + 208;
static_assert(L2_END <= 163840 && L2_WC + 4096 <= L2_E && L2_SRT % 16 == 0 && L2_GN % 8 == 0, "LDS map");
constexpr unsigned M_NEXT = L2_MISC, M_SLOW = L2_MISC + 8, M_INFO = L2_MISC + 12;
constexpr int NG = 25, OVL = EXP_OVL, NITEMS = 4 * NG;

struct Item { int w, t; };
__device__ __forceinline__ Item item_of(int i) {
  int w = i >= 2 * NG ? 2 : 0, r = i >= 2 * NG ? i - 2 * NG : i, m = -1;
  if (r >= NG) { r -= NG; w += 1; }
  if (r >= NG - OVL && w < 3) m = r - (NG - OVL);
  else if (r < OVL && w > 0) { m = r + OVL; w -= 1; }
  if (m >= 0) { if (m & 1) { w += 1; r = m >> 1; } else r = NG - OVL + (m >> 1); }
  return Item{w, r};
}
struct Tile { int ps, np, w, g, fl; };

#define DOT2(d, w, s, a) "v_dot2_f32_bf16 " d ", " w ", " s ", " a "\n\t"

__global__ void __launch_bounds__(1024) k2_edge(const uint16_t* __restrict__ x, const uint16_t* __restrict__ ea,
                                                const uint16_t* __restrict__ wa, const uint16_t* __restrict__ wu,
                                                char* __restrict__ ws, uint16_t* __restrict__ out) {
  __shared__ __attribute__((aligned(256))) char smem[L2_END];
  const unsigned sb = (unsigned)(size_t)(LAS char*)smem;
  const int tid = threadIdx.x, lane = tid & 63, wv = __builtin_amdgcn_readfirstlane(tid >> 6);
  const int bk = (blockIdx.x & 7) * 32 + (blockIdx.x >> 3), node0 = bk * NB;
  const int h = lane >> 5, c = lane & 31;
  STAMP(1, 0);
  const int seg = tid >> 2, sj = tid & 3;
  const u32x2 o2 = *(const u32x2*)(ws + WS_OFF + seg * OFFP + 4 * bk);
  if (tid < (L2_NI - L2_CNT) / 4) {
    const unsigned a = L2_CNT + 4 * tid;
    ldsw<unsigned>(sb + a, a == M_NEXT ? 16u : (a >= L2_SRT && a < L2_MISC) ? (unsigned)NB : 0u);
  }
  if (tid < CAPT * 8) {
    const unsigned dv = NN * 128 + 128 - 128 * (tid & 1);
    ldsw<u32x4>(sb + L2_SRC + 16 * tid, u32x4{dv, dv, dv, dv});
    ldsw<u32x4>(sb + L2_E + (tid >> 3) * 1024 + (tid & 7) * 16, u32x4{0, 0, 0, 0});
  }
  bar_lds();
  const int s0 = o2[0] & 0xffff, s1 = o2[0] >> 16, len = (o2[1] & 0xffff) - s0;
  const unsigned* recs = (const unsigned*)(ws + WS_REC) + seg * CH + s0;
  const bool big = __any(len > 16);
  u32x4 ra = {0, 0, 0, 0}, rb = {0, 0, 0, 0};
  if (4 * sj < len) ra = *(const u32x4*)(recs + 4 * sj);
  if (big) { if (16 + 4 * sj < len) rb = *(const u32x4*)(recs + 16 + 4 * sj); if (len > 32) ldsw<unsigned>(sb + M_SLOW, 1u); }
  STAMP(1, 1);
  unsigned na[4], ka[4], nb_[4], kb[4];
#pragma unroll
  for (int j = 0; j < 4; ++j) {
    const int i = 4 * sj + j;
    na[j] = (ra[j] >> 26) + (s0 + i >= s1 ? FB : 0); ka[j] = 0;
    if (i < len) ka[j] = lds_add_rtn(sb + L2_CNT + 4 * na[j], 1u);
  }
  if (big) {
#pragma unroll
    for (int j = 0; j < 4; ++j) {
      const int i = 16 + 4 * sj + j;
      nb_[j] = (rb[j] >> 26) + (s0 + i >= s1 ? FB : 0); kb[j] = 0;
      if (i < len) kb[j] = lds_add_rtn(sb + L2_CNT + 4 * nb_[j], 1u);
    }
  }
  u32x4 wcv = {0, 0, 0, 0};
  if (wv >= 12) wcv = *(const u32x4*)(ws + WS_WC + (tid - 768) * 16);
  stage_wx(wa, wu, x, node0, sb + L2_W, sb + L2_X, wv, lane);
  bar_lds();
  STAMP(1, 2);
  if (wv == 0) {
    unsigned k0 = 0, k1 = 0, b0 = 0, b1 = 0, r0 = 0, r1 = 0;
    if (lane < NB / 2) {
      const u32x2 cn = ldsr<u32x2>(sb + L2_CNT + 8 * lane);
      k0 = (cn[0] + 1) >> 1; k1 = (cn[1] + 1) >> 1;
      b0 = 63 - min(k0, 63u); b1 = 63 - min(k1, 63u);
      r0 = lds_add_rtn(sb + L2_HIST + 4 * b0, 1u); r1 = lds_add_rtn(sb + L2_HIST + 4 * b1, 1u);
    }
    const unsigned hv = __hip_atomic_load((LAS unsigned*)(sb + L2_HIST + 4 * lane), __ATOMIC_RELAXED, __HIP_MEMORY_SCOPE_WORKGROUP);
    const unsigned hb = wave_incl_scan(hv) - hv;
    const unsigned p0 = __builtin_amdgcn_ds_bpermute(4 * b0, hb) + r0, p1 = __builtin_amdgcn_ds_bpermute(4 * b1, hb) + r1;
    if (lane < NB / 2) { ldsw<unsigned>(sb + L2_SRT + 4 * p0, (2 * lane) | (k0 << 8)); ldsw<unsigned>(sb + L2_SRT + 4 * p1, (2 * lane + 1) | (k1 << 8)); }
    asm volatile("" ::: "memory");
    u32x4 q = {0, 0, 0, 0};
    unsigned gl = 0;
    if (lane < NG) { q = ldsr<u32x4>(sb + L2_SRT + 16 * lane); gl = max(max(max(q[0], q[1]), max(q[2], q[3])) >> 8, 1u); }
    const unsigned inc = wave_incl_scan(gl), gb = inc - gl;
    if (lane < NG) {
      ldsw<unsigned>(sb + L2_GI + 4 * lane, gb | (gl << 16));
      ldsw<u32x2>(sb + L2_GN + 8 * lane, u32x2{((q[0] & 255) << 8) | ((q[1] & 255) << 24), ((q[2] & 255) << 8) | ((q[3] & 255) << 24)});
#pragma unroll
      for (int j = 0; j < 4; ++j) ldsw<unsigned>(sb + L2_NI + 4 * (q[j] & 255), gb * 8 + (j >> 1) * 4 + (j & 1));
    }
    if (lane == 63) { ldsw<unsigned>(sb + M_INFO, inc); if (inc > CAPT * 4) ldsw<unsigned>(sb + M_SLOW, 1u); }
  }
  bar_lds();
  const u32x2 inf = ldsr<u32x2>(sb + M_SLOW);
  const bool slow = __builtin_amdgcn_readfirstlane(inf[0]) != 0;
  const int nte = slow ? 0 : (__builtin_amdgcn_readfirstlane(inf[1]) + 3) >> 2;
  auto place = [&](unsigned rec, unsigned node, unsigned k) __attribute__((always_inline)) {
    const unsigned L = ldsr<unsigned>(sb + L2_NI + 4 * node) + (k >> 1) * 8 + (k & 1) * 2;
    ldsw<unsigned>(sb + L2_SRC + 4 * L, ((rec >> 11) & 0x7fff) * 128 + 128 - 32 * (L & 4));
    ldsw<unsigned>(sb + L2_E + (L >> 5) * 1024 + (L & 31) * 4, (seg * CH + (rec & 0x7ff)) * 32);
  };
  if (!slow) {
#pragma unroll
    for (int j = 0; j < 4; ++j) if (4 * sj + j < len) place(ra[j], na[j], ka[j]);
    if (big) {
#pragma unroll
      for (int j = 0; j < 4; ++j) if (16 + 4 * sj + j < len) place(rb[j], nb_[j], kb[j]);
    }
  }
  bar_all();
  STAMP(1, 3);
  {
    const u32x4 se = make_srd(ea, NE * 32);
    unsigned eo[4];
#pragma unroll
    for (int j = 0; j < 4; ++j) { eo[j] = 0; if (wv + 16 * j < nte) eo[j] = ldsr<unsigned>(sb + L2_E + (wv + 16 * j) * 1024 + 4 * (lane >> 1)) + 16 * (lane & 1); }
#pragma unroll
    for (int j = 0; j < 4; ++j) if (wv + 16 * j < nte) dma16(se, eo[j], sb + L2_E + (wv + 16 * j) * 1024);
  }
  int s_ps = 0, s_rem = 0, s_w = 0, s_g = 0;
  auto open_item = [&](int idx) __attribute__((always_inline)) {
    const Item it = item_of(min(idx, NITEMS - 1));
    const unsigned gi = __builtin_amdgcn_readfirstlane(ldsr<unsigned>(sb + L2_GI + 4 * it.t));
    s_w = it.w; s_g = it.t; s_ps = gi & 0xffff; s_rem = idx < NITEMS ? gi >> 16 : 0;
  };
  auto take = [&](int fl) __attribute__((always_inline)) {
    const int np = min(s_rem, 4);
    const Tile t = {s_ps, np, s_w, s_g, fl | (s_rem == np ? 2 : 0)};
    s_ps += np; s_rem -= np;
    return t;
  };
  auto advance = [&]() __attribute__((always_inline)) {
    int fl = 0;
    if (s_rem == 0) { open_item(lds_inc_rtn(sb + M_NEXT)); fl = 1; }
    return take(fl);
  };
  auto issue = [&](const Tile& t, unsigned (&v)[16]) __attribute__((always_inline)) {
    const unsigned la = sb + L2_SRC + t.ps * 32 + h * 16;
    const int so = EXP_SAMEW ? 0 : t.w * WSTR;
#pragma unroll
    for (int u = 0; u < 4; ++u) {
      const auto rs = __builtin_amdgcn_make_buffer_rsrc(ws + WS_TAB - 128, (short)4, (u < t.np && !EXP_NOLOAD) ? 4 * WSTR + 128 : 0, 1 << 23);
      const u32x4 q = ldsr<u32x4>(la + 32 * u);
#pragma unroll
      for (int z = 0; z < 4; ++z) v[4 * u + z] = __builtin_amdgcn_raw_buffer_load_b32(rs, q[z], so, 0);
    }
  };
  unsigned va[16], vb[16];
  open_item(slow ? NITEMS : wv);
  if (slow) s_ps = 0;
  Tile ta = take(1);
  issue(ta, va);
  {
    const int rt = wv & 3, ow = wv >> 2;
    f32x16 cA, cU;
    gemm_tile(sb + L2_W, sb + L2_X, rt, ow, lane, cA, cU);
    const unsigned q = (lane & 15) >> 2, p = lane & 3, ch = 4 * ow + 2 * ((lane >> 4) & 1) + (p >> 1);
    unsigned ux[16];
#pragma unroll
    for (int g = 0; g < 4; ++g) {
      const unsigned row = 32 * rt + 8 * g + 4 * h + q;
      const u32x2 xx = tr_read(sb + L2_X + 256 * row + 16 * (ch ^ ((q << 2) | ((2 * g + h) & 3))) + 8 * (p & 1));
      const unsigned u01 = cvt_pk(cU[4 * g], cU[4 * g + 1]), u23 = cvt_pk(cU[4 * g + 2], cU[4 * g + 3]);
      ux[4 * g] = __builtin_amdgcn_perm(xx[0], u01, 0x05040100); ux[4 * g + 1] = __builtin_amdgcn_perm(xx[0], u01, 0x07060302);
      ux[4 * g + 2] = __builtin_amdgcn_perm(xx[1], u23, 0x05040100); ux[4 * g + 3] = __builtin_amdgcn_perm(xx[1], u23, 0x07060302);
    }
    bar_lds();
    STAMP(1, 4);
    const unsigned ab = (32 * rt + 4 * h) * 256 + (32 * ow + c) * 2;
    auto put_a = [&](int r) __attribute__((always_inline)) {
      const int rr = (r & 3) + 8 * (r >> 2);
      ldsw<uint16_t>(sb + L2_A + ab + rr * 256, (uint16_t)cvt_pk(cA[r] * NLOG2E, 0.f));
      ldsw<unsigned>(sb + L2_UX + 2 * ab + rr * 512, ux[r]);
    };
    if (rt < 3 || h == 0) { put_a(0); put_a(1); }
    if (rt < 3) {
#pragma unroll
      for (int r = 2; r < 16; ++r) put_a(r);
    }
    if (wv >= 12) ldsw<u32x4>(sb + L2_WC + (tid - 768) * 16, wcv);
  }
  asm volatile("s_waitcnt vmcnt(16) lgkmcnt(0)\n\ts_barrier" ::: "memory");
  STAMP(1, 5);

  const auto ro = __builtin_amdgcn_make_buffer_rsrc(out + (size_t)node0 * 128, (short)0, min(NB, NN - node0) * 256, 0x00020000);
  constexpr float EPS = 1e-6f * 0x1p30f;
  if (slow) {
    for (int w = 0; w < 4; ++w) {
      for (int i = tid; i < NB * 64; i += 1024) ldsw<unsigned>(sb + L2_E + 4 * i, 0u);
      bar_lds();
      const u32x4 w0 = ldsr<u32x4>(sb + L2_WC + w * 1024 + c * 16), w1 = ldsr<u32x4>(sb + L2_WC + w * 1024 + 512 + c * 16);
      for (int sg = wv; sg < 256; sg += 16) {
        const u32x2 oo = *(const u32x2*)(ws + WS_OFF + sg * OFFP + 4 * bk);
        const int b0 = oo[0] & 0xffff, b1 = oo[0] >> 16, b2 = oo[1] & 0xffff;
        for (int i = b0 + h; i < b2; i += 2) {
          const unsigned rec = ((const unsigned*)(ws + WS_REC))[sg * CH + i];
          const unsigned node = (rec >> 26) + (i >= b1 ? FB : 0), sn = (rec >> 11) & 0x7fff, eid = sg * CH + (rec & 0x7ff);
          const u32x4 e0 = *(const u32x4*)(ea + eid * 16), e1 = *(const u32x4*)(ea + eid * 16 + 8);
          const unsigned wd = *(const unsigned*)(ws + WS_TAB + (size_t)w * WSTR + sn * 128 + 4 * c);
          float gs = __uint_as_float((unsigned)ldsr<uint16_t>(sb + L2_A + node * 256 + (32 * w + c) * 2) << 16) + __uint_as_float(wd << 16);
#pragma unroll
          for (int j = 0; j < 4; ++j) {
            gs = __builtin_amdgcn_fdot2_f32_bf16(as_bf2(e0[j]), as_bf2(w0[j]), gs, false);
            gs = __builtin_amdgcn_fdot2_f32_bf16(as_bf2(e1[j]), as_bf2(w1[j]), gs, false);
          }
          const float eta = __builtin_amdgcn_rcpf(fmaf(__builtin_amdgcn_exp2f(gs), 0x1p-30f, 0x1p-30f));
          lds_addf(sb + L2_E + node * 256 + 4 * c, eta * __uint_as_float(wd)); lds_addf(sb + L2_E + node * 256 + 128 + 4 * c, eta);
        }
      }
      bar_lds();
      for (int n = 2 * wv + h; n < NB; n += 32) {
        const unsigned o = n * 256 + (32 * w + c) * 2, ux = ldsr<unsigned>(sb + L2_UX + 2 * o);
        const float z = fmaf(ldsr<float>(sb + L2_E + n * 256 + 4 * c), __builtin_amdgcn_rcpf(ldsr<float>(sb + L2_E + n * 256 + 128 + 4 * c) + EPS), __uint_as_float(ux << 16));
        __builtin_amdgcn_raw_buffer_store_b16((uint16_t)cvt_pk(__uint_as_float(ux & 0xffff0000u) + fmaxf(z, 0.f), 0.f), ro, o, 0, 0);
      }
      bar_lds();
    }
    return;
  }

  const unsigned one_lo = 0x00003f80u;
  unsigned o0 = 0, o1 = 0;
  f32x2 A2 = {0.f, 0.f}, num = {0.f, 0.f}, den = {0.f, 0.f};
  u32x4 wf = {0, 0, 0, 0};
  auto compute = [&](const Tile& t, unsigned (&v)[16]) __attribute__((always_inline)) {
    if (t.np == 0) return;
    const u32x4 ef = ldsr<u32x4>(sb + L2_E + t.ps * 256 + c * 32 + h * 16);
    if (t.fl & 1) {
      const unsigned nd = ldsr<unsigned>(sb + L2_GN + 8 * t.g + 4 * h), fo = (32 * t.w + c) * 2;
      wf = ldsr<u32x4>(sb + L2_WC + t.w * 1024 + lane * 16);
      o0 = (nd & 0xffff) + fo; o1 = (nd >> 16) + fo;
      A2 = f32x2{__uint_as_float((unsigned)ldsr<uint16_t>(sb + L2_A + o0) << 16), __uint_as_float((unsigned)ldsr<uint16_t>(sb + L2_A + o1) << 16)} - 60.f;
      num = f32x2{0.f, 0.f}; den = f32x2{0.f, 0.f};
    }
    float d[16];
#pragma unroll
    for (int u = 0; u < 4; ++u) {
      if (u == 0 || u < t.np)
        asm(DOT2("%0", "%4", "%8", "%9") DOT2("%1", "%5", "%8", "%10") DOT2("%2", "%6", "%8", "%9") DOT2("%3", "%7", "%8", "%10")
            : "=&v"(d[4 * u]), "=&v"(d[4 * u + 1]), "=&v"(d[4 * u + 2]), "=&v"(d[4 * u + 3])
            : "v"(v[4 * u]), "v"(v[4 * u + 1]), "v"(v[4 * u + 2]), "v"(v[4 * u + 3]), "s"(one_lo), "v"(A2[0]), "v"(A2[1]));
      else
        asm("" : "=v"(d[4 * u]), "=v"(d[4 * u + 1]), "=v"(d[4 * u + 2]), "=v"(d[4 * u + 3]));
    }
    f32x16 g;
#pragma unroll
    for (int r = 0; r < 16; ++r) g[r] = d[r];
    asm volatile("s_nop 2" : "+v"(g));
    g = mfma(ef, wf, g);
#pragma unroll
    for (int u = 0; u < 4; ++u) {
      if (u == 0 || u < t.np) {
        const f32x2 t0 = {__builtin_amdgcn_fmed3f(__builtin_amdgcn_exp2f(g[4 * u]), 0.f, 1.f), __builtin_amdgcn_fmed3f(__builtin_amdgcn_exp2f(g[4 * u + 1]), 0.f, 1.f)};
        const f32x2 t1 = {__builtin_amdgcn_fmed3f(__builtin_amdgcn_exp2f(g[4 * u + 2]), 0.f, 1.f), __builtin_amdgcn_fmed3f(__builtin_amdgcn_exp2f(g[4 * u + 3]), 0.f, 1.f)};
        const f32x2 a0 = __builtin_elementwise_fma(t0, f32x2{0x1p30f, 0x1p30f}, f32x2{0x1p-30f, 0x1p-30f});
        const f32x2 a1 = __builtin_elementwise_fma(t1, f32x2{0x1p30f, 0x1p30f}, f32x2{0x1p-30f, 0x1p-30f});
        const f32x2 v0 = {__uint_as_float(v[4 * u]), __uint_as_float(v[4 * u + 1])}, v1 = {__uint_as_float(v[4 * u + 2]), __uint_as_float(v[4 * u + 3])};
        const f32x2 p = a0 * a1, r = {__builtin_amdgcn_rcpf(p[0]), __builtin_amdgcn_rcpf(p[1])};
        num = __builtin_elementwise_fma(__builtin_elementwise_fma(v1, a0, v0 * a1), r, num);
        den = __builtin_elementwise_fma(a0 + a1, r, den);
      }
    }
    if (t.fl & 2) {
      const unsigned x0 = ldsr<unsigned>(sb + L2_UX + 2 * o0), x1 = ldsr<unsigned>(sb + L2_UX + 2 * o1);
      const f32x2 de = den + EPS, r = {__builtin_amdgcn_rcpf(de[0]), __builtin_amdgcn_rcpf(de[1])};
      const f32x2 z = __builtin_elementwise_fma(num, r, f32x2{__uint_as_float(x0 << 16), __uint_as_float(x1 << 16)});
      const unsigned res = cvt_pk(__uint_as_float(x0 & 0xffff0000u) + fmaxf(z[0], 0.f), __uint_as_float(x1 & 0xffff0000u) + fmaxf(z[1], 0.f));
      __builtin_amdgcn_raw_buffer_store_b16((uint16_t)res, ro, o0, 0, 0);
      __builtin_amdgcn_raw_buffer_store_b16((uint16_t)(res >> 16), ro, o1, 0, 0);
    }
  };
  while (ta.np > 0) {
    const Tile tb = advance();
    issue(tb, vb);
    compute(ta, va);
    ta = advance();
    issue(ta, va);
    compute(tb, vb);
  }
  STAMPW(1, 6);
  asm volatile("s_waitcnt vmcnt(0)" ::: "memory");
  STAMPW(1, 7);
}

extern "C" void kernel_launch(void* const* d_in, const int* in_sizes, int n_in, void* d_out, int out_size, void* d_ws, size_t ws_size,
                              hipStream_t stream) {
  const uint16_t* x = (const uint16_t*)d_in[0];
  const uint16_t* ea = (const uint16_t*)d_in[1];
  const int* src = (const int*)d_in[2];
  const int* dst = (const int*)d_in[3];
  k1_prep<<<256, 1024, 0, stream>>>(x, src, dst, (const uint16_t*)d_in[5], (const uint16_t*)d_in[8], (const uint16_t*)d_in[6], (char*)d_ws);
  k2_edge<<<256, 1024, 0, stream>>>(x, ea, (const uint16_t*)d_in[4], (const uint16_t*)d_in[7], (char*)d_ws, (uint16_t*)d_out);
  k3_amp<<<1, 64, 0, stream>>>((char*)d_ws);
}
```

```cpp
#define STAMPS
#define AMP_K0 0
#define AMP_I0 7
#define AMP_K 0
#define AMP_I 7
#define AMP_M 20
#define AMP_MODE 7
#include <hip/hip_runtime.h>
#include <stdint.h>
#pragma clang diagnostic ignored "-Wint-to-pointer-cast"

#define EXP_NOLOAD 0
#define EXP_SAMEW 0
#define EXP_OVL 3
#define LAS __attribute__((address_space(3)))
typedef unsigned u32x2 __attribute__((ext_vector_type(2)));
typedef unsigned u32x4 __attribute__((ext_vector_type(4)));
typedef float f32x2 __attribute__((ext_vector_type(2)));
typedef float f32x16 __attribute__((ext_vector_type(16)));
typedef short s16x4 __attribute__((ext_vector_type(4)));
typedef __bf16 bf16x8 __attribute__((ext_vector_type(8)));
typedef __bf16 bf16x2 __attribute__((ext_vector_type(2)));

constexpr int NN = 25000, NE = 400000;
constexpr int NB = 98;
constexpr int FB = 49;
constexpr int CH = 1568;
constexpr int OFFP = 1040;
constexpr int WSTR = 25088 * 128;
constexpr int CAPT = 61;
constexpr float NLOG2E = -1.4426950408889634f;

constexpr size_t WS_TAB = 4096;
constexpr size_t WS_REC = WS_TAB + 4 * (size_t)WSTR;
constexpr size_t WS_OFF = WS_REC + 256 * CH * 4 + 4096;
constexpr size_t WS_WC = WS_OFF + 256 * OFFP + 4096;
constexpr size_t WS_DBG = WS_WC + 4096;

#define STAMP(k, i) do { if ((threadIdx.x & 63) == 0) ((unsigned long long*)(ws + WS_DBG))[(((k) * 256 + blockIdx.x) * 8 + (i)) * 16 + (threadIdx.x >> 6)] = wall_clock64(); } while (0)
#define STAMPW STAMP
__global__ void k0_stamp(char* ws) { ((unsigned long long*)(ws + WS_DBG))[2 * 256 * 8 * 16] = wall_clock64(); }
#define KX_LDS 160000
#define KX_T 1024
__global__ void __launch_bounds__(1024) kx_stamp(char* ws) { __shared__ char sm[KX_LDS]; const unsigned long long t = wall_clock64();
  if (threadIdx.x < 16) ((unsigned long long*)(ws + WS_DBG))[((0 * 256 + blockIdx.x) * 8 + 7) * 16 + threadIdx.x] = t; if (t == 12345) sm[threadIdx.x] = 1; if (t == 12346) ws[0] = sm[t & 1023]; }
__global__ void k3_amp(char* ws) {
  const unsigned long long tk3 = wall_clock64();
  const unsigned long long* st = (const unsigned long long*)(ws + WS_DBG);
  unsigned long long lo = ~0ull, hi = 0;
  for (int i = threadIdx.x; i < 256 * 16; i += 64) {
    const unsigned long long a = st[((AMP_K0 * 256 + (i >> 4)) * 8 + AMP_I0) * 16 + (i & 15)], b = st[((AMP_K * 256 + (i >> 4)) * 8 + AMP_I) * 16 + (i & 15)];
    lo = a < lo ? a : lo; hi = b > hi ? b : hi;
  }
  for (int d = 32; d; d >>= 1) { const unsigned long long a = __shfl_xor(lo, d), b = __shfl_xor(hi, d); lo = a < lo ? a : lo; hi = b > hi ? b : hi; }
  unsigned long long dt = hi - lo;
  if (dt > 10000) dt = 10000;
  const unsigned long long t0 = wall_clock64();
  while (wall_clock64() - t0 < dt * AMP_M) __builtin_amdgcn_s_sleep(4);
}

__device__ __forceinline__ u32x4 make_srd(const void* p, unsigned bytes) {
  const uint64_t a = (uint64_t)p;
  return u32x4{(unsigned)a, (unsigned)(a >> 32) & 0xffffu, bytes, 0x00020000u};
}
__device__ __forceinline__ void dma16(u32x4 srd, unsigned voff, unsigned lds_dst) {
  unsigned keep;
  asm volatile("s_mov_b32 %0, m0\n\ts_mov_b32 m0, %3\n\ts_nop 0\n\tbuffer_load_dwordx4 %1, %2, 0 offen lds\n\ts_mov_b32 m0, %0"
               : "=&s"(keep) : "v"(voff), "s"(srd), "s"(lds_dst) : "memory");
}
__device__ __forceinline__ void bar_lds() { asm volatile("s_waitcnt lgkmcnt(0)\n\ts_barrier" ::: "memory"); }
__device__ __forceinline__ void bar_all() { asm volatile("s_waitcnt vmcnt(0) lgkmcnt(0)\n\ts_barrier" ::: "memory"); }
template <class T> __device__ __forceinline__ T ldsr(unsigned a) { return *(const LAS T*)a; }
template <class T> __device__ __forceinline__ void ldsw(unsigned a, T v) { *(LAS T*)a = v; }
__device__ __forceinline__ unsigned lds_add_rtn(unsigned a, unsigned v) {
  return __hip_atomic_fetch_add((LAS unsigned*)a, v, __ATOMIC_RELAXED, __HIP_MEMORY_SCOPE_WORKGROUP);
}
__device__ __forceinline__ void lds_addf(unsigned a, float v) {
  __hip_atomic_fetch_add((LAS float*)a, v, __ATOMIC_RELAXED, __HIP_MEMORY_SCOPE_WORKGROUP);
}
__device__ __forceinline__ unsigned lds_poll(unsigned a) {
  const unsigned v = __builtin_amdgcn_readfirstlane(__hip_atomic_load((LAS unsigned*)a, __ATOMIC_RELAXED, __HIP_MEMORY_SCOPE_WORKGROUP));
  asm volatile("" ::: "memory");
  return v;
}
__device__ __forceinline__ void lds_wait_ge(unsigned a, unsigned n) {
  for (int i = 0; i < (1 << 20) && lds_poll(a) < n; ++i) __builtin_amdgcn_s_sleep(1);
}
__device__ __forceinline__ int lds_inc_rtn(unsigned a) {
  unsigned r;
  asm volatile("s_mov_b64 exec, 1\n\tds_add_rtn_u32 %0, %1, %2\n\ts_mov_b64 exec, -1\n\ts_waitcnt lgkmcnt(0)" : "=&v"(r) : "v"(a), "v"(1u) : "memory");
  return __builtin_amdgcn_readfirstlane(r);
}
__device__ __forceinline__ void lds_inc(unsigned a) {
  asm volatile("s_mov_b64 exec, 1\n\tds_add_u32 %0, %1\n\ts_mov_b64 exec, -1" :: "v"(a), "v"(1u) : "memory");
}
__device__ __forceinline__ u32x2 tr_read(unsigned a) {
  return __builtin_bit_cast(u32x2, __builtin_amdgcn_ds_read_tr16_b64_v4i16((LAS s16x4*)a));
}
__device__ __forceinline__ unsigned cvt_pk(float lo, float hi) {
  return __builtin_bit_cast(unsigned, bf16x2{(__bf16)lo, (__bf16)hi});
}
__device__ __forceinline__ bf16x2 as_bf2(unsigned v) { return __builtin_bit_cast(bf16x2, v); }
__device__ __forceinline__ f32x16 mfma(u32x4 a, u32x4 b, f32x16 c) {
  return __builtin_amdgcn_mfma_f32_32x32x16_bf16(__builtin_bit_cast(bf16x8, a), __builtin_bit_cast(bf16x8, b), c, 0, 0, 0);
}
__device__ __forceinline__ unsigned wave_incl_scan(unsigned v) {
  v += __builtin_amdgcn_update_dpp(0u, v, 0x111, 0xf, 0xf, true);
  v += __builtin_amdgcn_update_dpp(0u, v, 0x112, 0xf, 0xf, true);
  v += __builtin_amdgcn_update_dpp(0u, v, 0x114, 0xf, 0xf, true);
  v += __builtin_amdgcn_update_dpp(0u, v, 0x118, 0xf, 0xf, true);
  v += __builtin_amdgcn_update_dpp(0u, v, 0x142, 0xa, 0xf, true);
  v += __builtin_amdgcn_update_dpp(0u, v, 0x143, 0xc, 0xf, true);
  return v;
}

__device__ __forceinline__ void stage_wx(const void* w0, const void* w1, const void* x, int node0, unsigned ldsW, unsigned ldsX, int wv, int lane) {
  const unsigned m = ((lane >> 4) << 2) | (wv & 3);
  const unsigned voff = (4 * wv + (lane >> 4)) * 256 + 16 * ((lane & 15) ^ m);
  const u32x4 sx = make_srd(x, NN * 256), s0 = make_srd(w0, 32768), s1 = make_srd(w1, 32768);
  dma16(sx, node0 * 256 + voff, ldsX + wv * 1024);
  if (wv < 9) { if (wv < 8 || lane < 32) dma16(sx, node0 * 256 + voff + 16384, ldsX + 16384 + wv * 1024); }
  dma16(s0, voff, ldsW + wv * 1024);
  dma16(s0, voff + 16384, ldsW + 16384 + wv * 1024);
  dma16(s1, voff, ldsW + 32768 + wv * 1024);
  dma16(s1, voff + 16384, ldsW + 49152 + wv * 1024);
}
__device__ __forceinline__ void gemm_tile(unsigned ldsW, unsigned ldsX, int rt, int cb, int lane, f32x16& c0, f32x16& c1) {
  const unsigned h = lane >> 5, blk = (lane >> 4) & 1, q = (lane & 15) >> 2, p = lane & 3;
  const unsigned row = 32 * rt + (lane & 31);
  const unsigned xa = ldsX + 256 * row + 16 * (h ^ (((row & 3) << 2) | ((row >> 2) & 3)));
  const unsigned ch = 4 * cb + 2 * blk + (p >> 1);
  const unsigned wa0 = ldsW + 256 * (8 * h + q) + 16 * (ch ^ ((q << 2) | (2 * h))) + 8 * (p & 1);
  const unsigned wa1 = ldsW + 256 * (8 * h + 4 + q) + 16 * (ch ^ ((q << 2) | (2 * h + 1))) + 8 * (p & 1);
#pragma unroll
  for (int i = 0; i < 16; ++i) { c0[i] = 0.f; c1[i] = 0.f; }
#pragma unroll
  for (int s = 0; s < 8; ++s) {
    const u32x4 a = ldsr<u32x4>(xa ^ (32 * s));
    const u32x2 b00 = tr_read(wa0 + 4096 * s), b01 = tr_read(wa1 + 4096 * s);
    const u32x2 b10 = tr_read(wa0 + 4096 * s + 32768), b11 = tr_read(wa1 + 4096 * s + 32768);
    c0 = mfma(a, u32x4{b00[0], b00[1], b01[0], b01[1]}, c0);
    c1 = mfma(a, u32x4{b10[0], b10[1], b11[0], b11[1]}, c1);
  }
}

constexpr unsigned L1_W = 0, L1_X = 65536, L1_CNT = 98304, L1_OFF = L1_CNT + 2048, L1_REC = L1_OFF + 1024, L1_END = L1_REC + CH * 4;

__global__ void __launch_bounds__(1024) k1_prep(const uint16_t* __restrict__ x, const int* __restrict__ src, const int* __restrict__ dst,
                                                const uint16_t* __restrict__ wb, const uint16_t* __restrict__ wv_, const uint16_t* __restrict__ wc,
                                                char* __restrict__ ws) {
  __shared__ __attribute__((aligned(256))) char smem[L1_END];
  const unsigned sb = (unsigned)(size_t)(LAS char*)smem;
  const int tid = threadIdx.x, lane = tid & 63, wv = __builtin_amdgcn_readfirstlane(tid >> 6);
  const int g = blockIdx.x;
  const int node0 = ((g & 7) * 32 + (g >> 3)) * NB;
  STAMP(0, 0);
  const int bt = tid - 512;
  const bool bin = bt >= 0 && bt < CH / 4 && g * CH + 4 * bt < NE;
  u32x4 d4 = {0, 0, 0, 0}, s4 = {0, 0, 0, 0};
  if (bin) { d4 = *(const u32x4*)(dst + g * CH + 4 * bt); s4 = *(const u32x4*)(src + g * CH + 4 * bt); }
  stage_wx(wb, wv_, x, node0, sb + L1_W, sb + L1_X, wv, lane);
  if (tid < 512) ldsw<unsigned>(sb + L1_CNT + 4 * tid, 0u);
  if (wv == 15 && g < 4) {
    unsigned f[4];
#pragma unroll
    for (int j = 0; j < 4; ++j) {
      const int k = 8 * (lane >> 5) + 2 * j, c = 32 * g + (lane & 31);
      const float a = __uint_as_float((unsigned)wc[k * 128 + c] << 16), b = __uint_as_float((unsigned)wc[k * 128 + 128 + c] << 16);
      f[j] = cvt_pk(a * NLOG2E, b * NLOG2E);
    }
    *(u32x4*)(ws + WS_WC + g * 1024 + lane * 16) = u32x4{f[0], f[1], f[2], f[3]};
    if (lane < 32) *(unsigned*)(ws + WS_TAB + (size_t)g * WSTR + NN * 128 + 4 * lane) = 0x00007f00u;
  }
  bar_lds();
  unsigned fine[4], rank[4];
  if (bin) {
#pragma unroll
    for (int j = 0; j < 4; ++j) { fine[j] = d4[j] / FB; rank[j] = lds_add_rtn(sb + L1_CNT + 4 * fine[j], 1u); }
  }
  bar_all();
  STAMP(0, 1);
  if (wv >= 8 && wv < 15) {
    const u32x4 a = ldsr<u32x4>(sb + L1_CNT + 32 * lane), b = ldsr<u32x4>(sb + L1_CNT + 32 * lane + 16);
    const unsigned p1 = a[0], p2 = p1 + a[1], p3 = p2 + a[2], p4 = p3 + a[3], p5 = p4 + b[0], p6 = p5 + b[1], p7 = p6 + b[2], tot = p7 + b[3];
    const unsigned inc = wave_incl_scan(tot), e = inc - tot;
    const u32x4 o = {e | ((e + p1) << 16), (e + p2) | ((e + p3) << 16), (e + p4) | ((e + p5) << 16), (e + p6) | ((e + p7) << 16)};
    ldsw<u32x4>(sb + L1_OFF + 16 * lane, o);
    if (wv == 8) {
      *(u32x4*)(ws + WS_OFF + g * OFFP + 16 * lane) = o;
      if (lane == 63) *(unsigned*)(ws + WS_OFF + g * OFFP + 1024) = inc;
    }
    if (bin) {
#pragma unroll
      for (int j = 0; j < 4; ++j) {
        const unsigned pos = ldsr<uint16_t>(sb + L1_OFF + 2 * fine[j]) + rank[j];
        ldsw<unsigned>(sb + L1_REC + 4 * pos, (unsigned)(4 * bt + j) | (s4[j] << 11) | ((d4[j] - fine[j] * FB) << 26));
      }
    }
  }
  {
    const int rt = wv & 3, cb = wv >> 2;
    f32x16 cB, cV;
    gemm_tile(sb + L1_W, sb + L1_X, rt, cb, lane, cB, cV);
    STAMP(0, 2);
    char* tab = ws + WS_TAB + (size_t)cb * WSTR + (size_t)(node0 + 32 * rt + 4 * (lane >> 5)) * 128 + (lane & 31) * 4;
    const int lim = min(NB, NN - node0) - 32 * rt - 4 * (lane >> 5);
#pragma unroll
    for (int r = 0; r < 16; ++r) {
      const int rr = (r & 3) + 8 * (r >> 2);
      const unsigned lo = cvt_pk(cB[r] * NLOG2E, 0.f);
      const int u = __float_as_int(cV[r]);
      int t = __builtin_elementwise_add_sat(u, (int)(0x8000u - lo));
      t = max(t, u & (int)0x80000000);
      if (rr < lim) *(unsigned*)(tab + rr * 128) = ((unsigned)t & 0xffff0000u) | lo;
    }
  }
  bar_lds();
  if (bin) *(u32x4*)(ws + WS_REC + (size_t)(g * CH + 4 * bt) * 4) = ldsr<u32x4>(sb + L1_REC + 16 * bt);
  bar_all();
  STAMP(0, 3);
}

constexpr unsigned L2_A = 0, L2_UX = 99 * 256, L2_WC = L2_UX + 99 * 512,
                   L2_W = 0, L2_X = 65536,
                   L2_E = L2_X + NB * 256, L2_SRC = L2_E + CAPT * 1024, L2_CNT = L2_SRC + CAPT * 128, L2_HIST = L2_CNT + 400,
                   L2_SRT = L2_HIST + 256, L2_MISC = L2_SRT + 400, L2_NI = L2_MISC + 16, L2_GI = L2_NI + 400, L2_GN = L2_GI + 112,
                   L2_END = L2_GN + 208;
static_assert(L2_END <= 163840 && L2_WC + 4096 <= L2_E && L2_SRT % 16 == 0 && L2_GN % 8 == 0, "LDS map");
constexpr unsigned M_NEXT = L2_MISC, M_SLOW = L2_MISC + 8, M_INFO = L2_MISC + 12;
constexpr int NG = 25, OVL = EXP_OVL, NITEMS = 4 * NG;

struct Item { int w, t; };
__device__ __forceinline__ Item item_of(int i) {
  int w = i >= 2 * NG ? 2 : 0, r = i >= 2 * NG ? i - 2 * NG : i, m = -1;
  if (r >= NG) { r -= NG; w += 1; }
  if (r >= NG - OVL && w < 3) m = r - (NG - OVL);
  else if (r < OVL && w > 0) { m = r + OVL; w -= 1; }
  if (m >= 0) { if (m & 1) { w += 1; r = m >> 1; } else r = NG - OVL + (m >> 1); }
  return Item{w, r};
}
struct Tile { int ps, np, w, g, fl; };

#define DOT2(d, w, s, a) "v_dot2_f32_bf16 " d ", " w ", " s ", " a "\n\t"

__global__ void __launch_bounds__(1024) k2_edge(const uint16_t* __restrict__ x, const uint16_t* __restrict__ ea,
                                                const uint16_t* __restrict__ wa, const uint16_t* __restrict__ wu,
                                                char* __restrict__ ws, uint16_t* __restrict__ out) {
  __shared__ __attribute__((aligned(256))) char smem[L2_END];
  const unsigned sb = (unsigned)(size_t)(LAS char*)smem;
  const int tid = threadIdx.x, lane = tid & 63, wv = __builtin_amdgcn_readfirstlane(tid >> 6);
  const int bk = (blockIdx.x & 7) * 32 + (blockIdx.x >> 3), node0 = bk * NB;
  const int h = lane >> 5, c = lane & 31;
  STAMP(1, 0);
  const int seg = tid >> 2, sj = tid & 3;
  const u32x2 o2 = *(const u32x2*)(ws + WS_OFF + seg * OFFP + 4 * bk);
  if (tid < (L2_NI - L2_CNT) / 4) {
    const unsigned a = L2_CNT + 4 * tid;
    ldsw<unsigned>(sb + a, a == M_NEXT ? 16u : (a >= L2_SRT && a < L2_MISC) ? (unsigned)NB : 0u);
  }
  if (tid < CAPT * 8) {
    const unsigned dv = NN * 128 + 128 - 128 * (tid & 1);
    ldsw<u32x4>(sb + L2_SRC + 16 * tid, u32x4{dv, dv, dv, dv});
    ldsw<u32x4>(sb + L2_E + (tid >> 3) * 1024 + (tid & 7) * 16, u32x4{0, 0, 0, 0});
  }
  bar_lds();
  const int s0 = o2[0] & 0xffff, s1 = o2[0] >> 16, len = (o2[1] & 0xffff) - s0;
  const unsigned* recs = (const unsigned*)(ws + WS_REC) + seg * CH + s0;
  const bool big = __any(len > 16);
  u32x4 ra = {0, 0, 0, 0}, rb = {0, 0, 0, 0};
  if (4 * sj < len) ra = *(const u32x4*)(recs + 4 * sj);
  if (big) { if (16 + 4 * sj < len) rb = *(const u32x4*)(recs + 16 + 4 * sj); if (len > 32) ldsw<unsigned>(sb + M_SLOW, 1u); }
  STAMP(1, 1);
  unsigned na[4], ka[4], nb_[4], kb[4];
#pragma unroll
  for (int j = 0; j < 4; ++j) {
    const int i = 4 * sj + j;
    na[j] = (ra[j] >> 26) + (s0 + i >= s1 ? FB : 0); ka[j] = 0;
    if (i < len) ka[j] = lds_add_rtn(sb + L2_CNT + 4 * na[j], 1u);
  }
  if (big) {
#pragma unroll
    for (int j = 0; j < 4; ++j) {
      const int i = 16 + 4 * sj + j;
      nb_[j] = (rb[j] >> 26) + (s0 + i >= s1 ? FB : 0); kb[j] = 0;
      if (i < len) kb[j] = lds_add_rtn(sb + L2_CNT + 4 * nb_[j], 1u);
    }
  }
  u32x4 wcv = {0, 0, 0, 0};
  if (wv >= 12) wcv = *(const u32x4*)(ws + WS_WC + (tid - 768) * 16);
  stage_wx(wa, wu, x, node0, sb + L2_W, sb + L2_X, wv, lane);
  bar_lds();
  STAMP(1, 2);
  if (wv == 0) {
    unsigned k0 = 0, k1 = 0, b0 = 0, b1 = 0, r0 = 0, r1 = 0;
    if (lane < NB / 2) {
      const u32x2 cn = ldsr<u32x2>(sb + L2_CNT + 8 * lane);
      k0 = (cn[0] + 1) >> 1; k1 = (cn[1] + 1) >> 1;
      b0 = 63 - min(k0, 63u); b1 = 63 - min(k1, 63u);
      r0 = lds_add_rtn(sb + L2_HIST + 4 * b0, 1u); r1 = lds_add_rtn(sb + L2_HIST + 4 * b1, 1u);
    }
    const unsigned hv = __hip_atomic_load((LAS unsigned*)(sb + L2_HIST + 4 * lane), __ATOMIC_RELAXED, __HIP_MEMORY_SCOPE_WORKGROUP);
    const unsigned hb = wave_incl_scan(hv) - hv;
    const unsigned p0 = __builtin_amdgcn_ds_bpermute(4 * b0, hb) + r0, p1 = __builtin_amdgcn_ds_bpermute(4 * b1, hb) + r1;
    if (lane < NB / 2) { ldsw<unsigned>(sb + L2_SRT + 4 * p0, (2 * lane) | (k0 << 8)); ldsw<unsigned>(sb + L2_SRT + 4 * p1, (2 * lane + 1) | (k1 << 8)); }
    asm volatile("" ::: "memory");
    u32x4 q = {0, 0, 0, 0};
    unsigned gl = 0;
    if (lane < NG) { q = ldsr<u32x4>(sb + L2_SRT + 16 * lane); gl = max(max(max(q[0], q[1]), max(q[2], q[3])) >> 8, 1u); }
    const unsigned inc = wave_incl_scan(gl), gb = inc - gl;
    if (lane < NG) {
      ldsw<unsigned>(sb + L2_GI + 4 * lane, gb | (gl << 16));
      ldsw<u32x2>(sb + L2_GN + 8 * lane, u32x2{((q[0] & 255) << 8) | ((q[1] & 255) << 24), ((q[2] & 255) << 8) | ((q[3] & 255) << 24)});
#pragma unroll
      for (int j = 0; j < 4; ++j) ldsw<unsigned>(sb + L2_NI + 4 * (q[j] & 255), gb * 8 + (j >> 1) * 4 + (j & 1));
    }
    if (lane == 63) { ldsw<unsigned>(sb + M_INFO, inc); if (inc > CAPT * 4) ldsw<unsigned>(sb + M_SLOW, 1u); }
  }
  bar_lds();
  const u32x2 inf = ldsr<u32x2>(sb + M_SLOW);
  const bool slow = __builtin_amdgcn_readfirstlane(inf[0]) != 0;
  const int nte = slow ? 0 : (__builtin_amdgcn_readfirstlane(inf[1]) + 3) >> 2;
  auto place = [&](unsigned rec, unsigned node, unsigned k) __attribute__((always_inline)) {
    const unsigned L = ldsr<unsigned>(sb + L2_NI + 4 * node) + (k >> 1) * 8 + (k & 1) * 2;
    ldsw<unsigned>(sb + L2_SRC + 4 * L, ((rec >> 11) & 0x7fff) * 128 + 128 - 32 * (L & 4));
    ldsw<unsigned>(sb + L2_E + (L >> 5) * 1024 + (L & 31) * 4, (seg * CH + (rec & 0x7ff)) * 32);
  };
  if (!slow) {
#pragma unroll
    for (int j = 0; j < 4; ++j) if (4 * sj + j < len) place(ra[j], na[j], ka[j]);
    if (big) {
#pragma unroll
      for (int j = 0; j < 4; ++j) if (16 + 4 * sj + j < len) place(rb[j], nb_[j], kb[j]);
    }
  }
  bar_all();
  STAMP(1, 3);
  {
    const u32x4 se = make_srd(ea, NE * 32);
    unsigned eo[4];
#pragma unroll
    for (int j = 0; j < 4; ++j) { eo[j] = 0; if (wv + 16 * j < nte) eo[j] = ldsr<unsigned>(sb + L2_E + (wv + 16 * j) * 1024 + 4 * (lane >> 1)) + 16 * (lane & 1); }
#pragma unroll
    for (int j = 0; j < 4; ++j) if (wv + 16 * j < nte) dma16(se, eo[j], sb + L2_E + (wv + 16 * j) * 1024);
  }
  int s_ps = 0, s_rem = 0, s_w = 0, s_g = 0;
  auto open_item = [&](int idx) __attribute__((always_inline)) {
    const Item it = item_of(min(idx, NITEMS - 1));
    const unsigned gi = __builtin_amdgcn_readfirstlane(ldsr<unsigned>(sb + L2_GI + 4 * it.t));
    s_w = it.w; s_g = it.t; s_ps = gi & 0xffff; s_rem = idx < NITEMS ? gi >> 16 : 0;
  };
  auto take = [&](int fl) __attribute__((always_inline)) {
    const int np = min(s_rem, 4);
    const Tile t = {s_ps, np, s_w, s_g, fl | (s_rem == np ? 2 : 0)};
    s_ps += np; s_rem -= np;
    return t;
  };
  auto advance = [&]() __attribute__((always_inline)) {
    int fl = 0;
    if (s_rem == 0) { open_item(lds_inc_rtn(sb + M_NEXT)); fl = 1; }
    return take(fl);
  };
  auto issue = [&](const Tile& t, unsigned (&v)[16]) __attribute__((always_inline)) {
    const unsigned la = sb + L2_SRC + t.ps * 32 + h * 16;
    const int so = EXP_SAMEW ? 0 : t.w * WSTR;
#pragma unroll
    for (int u = 0; u < 4; ++u) {
      const auto rs = __builtin_amdgcn_make_buffer_rsrc(ws + WS_TAB - 128, (short)4, (u < t.np && !EXP_NOLOAD) ? 4 * WSTR + 128 : 0, 1 << 23);
      const u32x4 q = ldsr<u32x4>(la + 32 * u);
#pragma unroll
      for (int z = 0; z < 4; ++z) v[4 * u + z] = __builtin_amdgcn_raw_buffer_load_b32(rs, q[z], so, 0);
    }
  };
  unsigned va[16], vb[16];
  open_item(slow ? NITEMS : wv);
  if (slow) s_ps = 0;
  Tile ta = take(1);
  issue(ta, va);
  {
    const int rt = wv & 3, ow = wv >> 2;
    f32x16 cA, cU;
    gemm_tile(sb + L2_W, sb + L2_X, rt, ow, lane, cA, cU);
    const unsigned q = (lane & 15) >> 2, p = lane & 3, ch = 4 * ow + 2 * ((lane >> 4) & 1) + (p >> 1);
    unsigned ux[16];
#pragma unroll
    for (int g = 0; g < 4; ++g) {
      const unsigned row = 32 * rt + 8 * g + 4 * h + q;
      const u32x2 xx = tr_read(sb + L2_X + 256 * row + 16 * (ch ^ ((q << 2) | ((2 * g + h) & 3))) + 8 * (p & 1));
      const unsigned u01 = cvt_pk(cU[4 * g], cU[4 * g + 1]), u23 = cvt_pk(cU[4 * g + 2], cU[4 * g + 3]);
      ux[4 * g] = __builtin_amdgcn_perm(xx[0], u01, 0x05040100); ux[4 * g + 1] = __builtin_amdgcn_perm(xx[0], u01, 0x07060302);
      ux[4 * g + 2] = __builtin_amdgcn_perm(xx[1], u23, 0x05040100); ux[4 * g + 3] = __builtin_amdgcn_perm(xx[1], u23, 0x07060302);
    }
    bar_lds();
    STAMP(1, 4);
    const unsigned ab = (32 * rt + 4 * h) * 256 + (32 * ow + c) * 2;
    auto put_a = [&](int r) __attribute__((always_inline)) {
      const int rr = (r & 3) + 8 * (r >> 2);
      ldsw<uint16_t>(sb + L2_A + ab + rr * 256, (uint16_t)cvt_pk(cA[r] * NLOG2E, 0.f));
      ldsw<unsigned>(sb + L2_UX + 2 * ab + rr * 512, ux[r]);
    };
    if (rt < 3 || h == 0) { put_a(0); put_a(1); }
    if (rt < 3) {
#pragma unroll
      for (int r = 2; r < 16; ++r) put_a(r);
    }
    if (wv >= 12) ldsw<u32x4>(sb + L2_WC + (tid - 768) * 16, wcv);
  }
  asm volatile("s_waitcnt vmcnt(16) lgkmcnt(0)\n\ts_barrier" ::: "memory");
  STAMP(1, 5);

  const auto ro = __builtin_amdgcn_make_buffer_rsrc(out + (size_t)node0 * 128, (short)0, min(NB, NN - node0) * 256, 0x00020000);
  constexpr float EPS = 1e-6f * 0x1p30f;
  if (slow) {
    for (int w = 0; w < 4; ++w) {
      for (int i = tid; i < NB * 64; i += 1024) ldsw<unsigned>(sb + L2_E + 4 * i, 0u);
      bar_lds();
      const u32x4 w0 = ldsr<u32x4>(sb + L2_WC + w * 1024 + c * 16), w1 = ldsr<u32x4>(sb + L2_WC + w * 1024 + 512 + c * 16);
      for (int sg = wv; sg < 256; sg += 16) {
        const u32x2 oo = *(const u32x2*)(ws + WS_OFF + sg * OFFP + 4 * bk);
        const int b0 = oo[0] & 0xffff, b1 = oo[0] >> 16, b2 = oo[1] & 0xffff;
        for (int i = b0 + h; i < b2; i += 2) {
          const unsigned rec = ((const unsigned*)(ws + WS_REC))[sg * CH + i];
          const unsigned node = (rec >> 26) + (i >= b1 ? FB : 0), sn = (rec >> 11) & 0x7fff, eid = sg * CH + (rec & 0x7ff);
          const u32x4 e0 = *(const u32x4*)(ea + eid * 16), e1 = *(const u32x4*)(ea + eid * 16 + 8);
          const unsigned wd = *(const unsigned*)(ws + WS_TAB + (size_t)w * WSTR + sn * 128 + 4 * c);
          float gs = __uint_as_float((unsigned)ldsr<uint16_t>(sb + L2_A + node * 256 + (32 * w + c) * 2) << 16) + __uint_as_float(wd << 16);
#pragma unroll
          for (int j = 0; j < 4; ++j) {
            gs = __builtin_amdgcn_fdot2_f32_bf16(as_bf2(e0[j]), as_bf2(w0[j]), gs, false);
            gs = __builtin_amdgcn_fdot2_f32_bf16(as_bf2(e1[j]), as_bf2(w1[j]), gs, false);
          }
          const float eta = __builtin_amdgcn_rcpf(fmaf(__builtin_amdgcn_exp2f(gs), 0x1p-30f, 0x1p-30f));
          lds_addf(sb + L2_E + node * 256 + 4 * c, eta * __uint_as_float(wd)); lds_addf(sb + L2_E + node * 256 + 128 + 4 * c, eta);
        }
      }
      bar_lds();
      for (int n = 2 * wv + h; n < NB; n += 32) {
        const unsigned o = n * 256 + (32 * w + c) * 2, ux = ldsr<unsigned>(sb + L2_UX + 2 * o);
        const float z = fmaf(ldsr<float>(sb + L2_E + n * 256 + 4 * c), __builtin_amdgcn_rcpf(ldsr<float>(sb + L2_E + n * 256 + 128 + 4 * c) + EPS), __uint_as_float(ux << 16));
        __builtin_amdgcn_raw_buffer_store_b16((uint16_t)cvt_pk(__uint_as_float(ux & 0xffff0000u) + fmaxf(z, 0.f), 0.f), ro, o, 0, 0);
      }
      bar_lds();
    }
    return;
  }

  const unsigned one_lo = 0x00003f80u;
  unsigned o0 = 0, o1 = 0;
  f32x2 A2 = {0.f, 0.f}, num = {0.f, 0.f}, den = {0.f, 0.f};
  u32x4 wf = {0, 0, 0, 0};
  auto compute = [&](const Tile& t, unsigned (&v)[16]) __attribute__((always_inline)) {
    if (t.np == 0) return;
    const u32x4 ef = ldsr<u32x4>(sb + L2_E + t.ps * 256 + c * 32 + h * 16);
    if (t.fl & 1) {
      const unsigned nd = ldsr<unsigned>(sb + L2_GN + 8 * t.g + 4 * h), fo = (32 * t.w + c) * 2;
      wf = ldsr<u32x4>(sb + L2_WC + t.w * 1024 + lane * 16);
      o0 = (nd & 0xffff) + fo; o1 = (nd >> 16) + fo;
      A2 = f32x2{__uint_as_float((unsigned)ldsr<uint16_t>(sb + L2_A + o0) << 16), __uint_as_float((unsigned)ldsr<uint16_t>(sb + L2_A + o1) << 16)} - 60.f;
      num = f32x2{0.f, 0.f}; den = f32x2{0.f, 0.f};
    }
    float d[16];
#pragma unroll
    for (int u = 0; u < 4; ++u) {
      if (u == 0 || u < t.np)
        asm(DOT2("%0", "%4", "%8", "%9") DOT2("%1", "%5", "%8", "%10") DOT2("%2", "%6", "%8", "%9") DOT2("%3", "%7", "%8", "%10")
            : "=&v"(d[4 * u]), "=&v"(d[4 * u + 1]), "=&v"(d[4 * u + 2]), "=&v"(d[4 * u + 3])
            : "v"(v[4 * u]), "v"(v[4 * u + 1]), "v"(v[4 * u + 2]), "v"(v[4 * u + 3]), "s"(one_lo), "v"(A2[0]), "v"(A2[1]));
      else
        asm("" : "=v"(d[4 * u]), "=v"(d[4 * u + 1]), "=v"(d[4 * u + 2]), "=v"(d[4 * u + 3]));
    }
    f32x16 g;
#pragma unroll
    for (int r = 0; r < 16; ++r) g[r] = d[r];
    asm volatile("s_nop 2" : "+v"(g));
    g = mfma(ef, wf, g);
#pragma unroll
    for (int u = 0; u < 4; ++u) {
      if (u == 0 || u < t.np) {
        const f32x2 t0 = {__builtin_amdgcn_fmed3f(__builtin_amdgcn_exp2f(g[4 * u]), 0.f, 1.f), __builtin_amdgcn_fmed3f(__builtin_amdgcn_exp2f(g[4 * u + 1]), 0.f, 1.f)};
        const f32x2 t1 = {__builtin_amdgcn_fmed3f(__builtin_amdgcn_exp2f(g[4 * u + 2]), 0.f, 1.f), __builtin_amdgcn_fmed3f(__builtin_amdgcn_exp2f(g[4 * u + 3]), 0.f, 1.f)};
        const f32x2 a0 = __builtin_elementwise_fma(t0, f32x2{0x1p30f, 0x1p30f}, f32x2{0x1p-30f, 0x1p-30f});
        const f32x2 a1 = __builtin_elementwise_fma(t1, f32x2{0x1p30f, 0x1p30f}, f32x2{0x1p-30f, 0x1p-30f});
        const f32x2 v0 = {__uint_as_float(v[4 * u]), __uint_as_float(v[4 * u + 1])}, v1 = {__uint_as_float(v[4 * u + 2]), __uint_as_float(v[4 * u + 3])};
        const f32x2 p = a0 * a1, r = {__builtin_amdgcn_rcpf(p[0]), __builtin_amdgcn_rcpf(p[1])};
        num = __builtin_elementwise_fma(__builtin_elementwise_fma(v1, a0, v0 * a1), r, num);
        den = __builtin_elementwise_fma(a0 + a1, r, den);
      }
    }
    if (t.fl & 2) {
      const unsigned x0 = ldsr<unsigned>(sb + L2_UX + 2 * o0), x1 = ldsr<unsigned>(sb + L2_UX + 2 * o1);
      const f32x2 de = den + EPS, r = {__builtin_amdgcn_rcpf(de[0]), __builtin_amdgcn_rcpf(de[1])};
      const f32x2 z = __builtin_elementwise_fma(num, r, f32x2{__uint_as_float(x0 << 16), __uint_as_float(x1 << 16)});
      const unsigned res = cvt_pk(__uint_as_float(x0 & 0xffff0000u) + fmaxf(z[0], 0.f), __uint_as_float(x1 & 0xffff0000u) + fmaxf(z[1], 0.f));
      __builtin_amdgcn_raw_buffer_store_b16((uint16_t)res, ro, o0, 0, 0);
      __builtin_amdgcn_raw_buffer_store_b16((uint16_t)(res >> 16), ro, o1, 0, 0);
    }
  };
  while (ta.np > 0) {
    const Tile tb = advance();
    issue(tb, vb);
    compute(ta, va);
    ta = advance();
    issue(ta, va);
    compute(tb, vb);
  }
  STAMPW(1, 6);
  asm volatile("s_waitcnt vmcnt(0)" ::: "memory");
  STAMPW(1, 7);
}

extern "C" void kernel_launch(void* const* d_in, const int* in_sizes, int n_in, void* d_out, int out_size, void* d_ws, size_t ws_size,
                              hipStream_t stream) {
  const uint16_t* x = (const uint16_t*)d_in[0];
  const uint16_t* ea = (const uint16_t*)d_in[1];
  const int* src = (const int*)d_in[2];
  const int* dst = (const int*)d_in[3];
  k1_prep<<<256, 1024, 0, stream>>>(x, src, dst, (const uint16_t*)d_in[5], (const uint16_t*)d_in[8], (const uint16_t*)d_in[6], (char*)d_ws);
  k2_edge<<<256, 1024, 0, stream>>>(x, ea, (const uint16_t*)d_in[4], (const uint16_t*)d_in[7], (char*)d_ws, (uint16_t*)d_out);
  kx_stamp<<<256, KX_T, 0, stream>>>((char*)d_ws);
  k3_amp<<<1, 64, 0, stream>>>((char*)d_ws);
}
```

```cpp
#include <hip/hip_runtime.h>
#include <stdint.h>
#pragma clang diagnostic ignored "-Wint-to-pointer-cast"

#define LAS __attribute__((address_space(3)))
#define INL __attribute__((always_inline))
typedef unsigned u32x2 __attribute__((ext_vector_type(2)));
typedef unsigned u32x4 __attribute__((ext_vector_type(4)));
typedef unsigned u32x8 __attribute__((ext_vector_type(8)));
typedef float f32x16 __attribute__((ext_vector_type(16)));
typedef short s16x4 __attribute__((ext_vector_type(4)));
typedef __bf16 bf16x8 __attribute__((ext_vector_type(8)));
typedef __bf16 bf16x2 __attribute__((ext_vector_type(2)));

constexpr int NN = 25000, NE = 400000;
constexpr int NB = 98;
constexpr int CH = 1568;
constexpr int WSTR = 25088 * 128;
constexpr float NLOG2E = -1.4426950408889634f;
constexpr float EPS = 1e-6f * 0x1p30f;
constexpr unsigned EPS_BITS = __builtin_bit_cast(unsigned, EPS);
constexpr int DEV = 16;
constexpr int SPIN = 1 << 16;
constexpr int PACE_BV = 3, PACE_AU = 2, FIRST_LOOK = 40, POLL = 6;

constexpr unsigned H_TAB = 0, H_TAG = 32768, H_END = H_TAG + 1024;
constexpr size_t WS_TAB = 36864, WS_REC = WS_TAB + 4 * (size_t)WSTR, WS_SEG = WS_REC + 256 * CH * 4 + 4096, WS_EP = WS_SEG + 512 * 1024;
__device__ __forceinline__ unsigned seg_at(unsigned g) { return (g >> 3) * 8192 + (g & 7) * 16; }

constexpr int SCAP = 1952, ECAP = 1796;
constexpr int NG = 25, QLEN = 40;
constexpr unsigned L_X = 0, L_W = NB * 256, L_A = L_W, L_WC = L_A + 99 * 256, L_U = L_W + 32768,
                   L_E = L_W + 65536,
                   L_SRC = L_E + (ECAP + 1) * 32,
                   L_EI = L_SRC + SCAP * 4,
                   L_CNT = L_EI + SCAP * 2, L_HIST = L_CNT + 512, L_IT = L_HIST + 256, L_SY = L_IT + 4 * QLEN * 4, L_BMP = L_SY + 112,
                   L_GC = L_BMP + 64,
                   L_NI = L_GC + 112, L_GN = L_NI + 800, L_G2 = L_GN + 208, L_SRT = L_G2 + 400, L_EINF = L_SRT + 512,
                   L_JUNK = L_EINF + 32, L_END = L_JUNK + 256;
constexpr unsigned L1_CNT = L_SRC, L1_INV = L1_CNT + 1024, L1_REC = L1_INV + CH * 2, L1_SEG = L_NI, L1_T = L_E + CH * 32;
static_assert(L_END <= 163840 && L_WC + 4096 <= L_U && L_U < 65536 && L1_REC + CH * 4 <= L_CNT && L1_SEG + 1024 <= L_EINF && CH + 128 <= ECAP, "LDS map");
static_assert((L_WC | L_SRC | L_EI | L_CNT | L_SY | L_G2 | L_SRT | L1_REC | L1_SEG) % 16 == 0 && L_GN % 8 == 0, "LDS alignment");
constexpr unsigned S_ETOP = L_SY, S_SLOW = L_SY + 4, S_RANKED = L_SY + 8, S_SORTED = L_SY + 12, S_NEXT = L_SY + 16, S_AU = L_SY + 32,
                   S_BIN = L_SY + 40, S_EIN = L_SY + 44, S_EOUT = L_SY + 48, S_EDONE = L_SY + 52, S_TAGGED = L_SY + 56, S_PAD = L_SY + 60,
                   S_GEMM = L_SY + 68, S_TDONE = L_SY + 72, S_SEEN = L_SY + 76, S_STAGED = L_SY + 80, S_TAG = L_SY + 84, S_TBUF = L_SY + 88,
                   S_LOOK = L_SY + 92, S_LIST = L_SY + 96, S_DATA = L_GC + 100, S_GO = L_GC + 104;
__device__ __forceinline__ unsigned rowbase(unsigned r) { return 256 * r + 16 * (((r & 3) << 2) | ((r >> 2) & 3)); }

__device__ __forceinline__ u32x4 make_srd(const void* p, unsigned bytes) {
  const uint64_t a = (uint64_t)p;
  return u32x4{(unsigned)a, (unsigned)(a >> 32) & 0xffffu, bytes, 0x00020000u};
}
__device__ __forceinline__ auto make_rsrc(void* p, int bytes) { return __builtin_amdgcn_make_buffer_rsrc(p, (short)0, bytes, 0x00020000); }
#define DMA(I) asm volatile("s_mov_b32 %0, m0\n\ts_mov_b32 m0, %3\n\ts_nop 0\n\t" I " lds\n\ts_mov_b32 m0, %0" \
                            : "=&s"(keep) : "v"(voff), "s"(srd), "s"(lds_dst) : "memory")
__device__ __forceinline__ void dma4(u32x4 srd, unsigned voff, unsigned lds_dst) { unsigned keep; DMA("buffer_load_dword %1, %2, 0 offen"); }
__device__ __forceinline__ void dma16(u32x4 srd, unsigned voff, unsigned lds_dst) { unsigned keep; DMA("buffer_load_dwordx4 %1, %2, 0 offen"); }
__device__ __forceinline__ void dma16_dev(u32x4 srd, unsigned voff, unsigned lds_dst) { unsigned keep; DMA("buffer_load_dwordx4 %1, %2, 0 offen sc1"); }
__device__ __forceinline__ void dma64(u32x4 srd, unsigned v0, unsigned v1, unsigned v2, unsigned v3, unsigned lds_dst) {
  unsigned keep;
  asm volatile("s_mov_b32 %0, m0\n\ts_mov_b32 m0, %6\n\ts_nop 0\n\tbuffer_load_dwordx4 %1, %5, 0 offen lds\n\tbuffer_load_dwordx4 %2, %5, 0 offen offset:1024 lds\n\t"
               "buffer_load_dwordx4 %3, %5, 0 offen offset:2048 lds\n\tbuffer_load_dwordx4 %4, %5, 0 offen offset:3072 lds\n\ts_mov_b32 m0, %0"
               : "=&s"(keep) : "v"(v0), "v"(v1), "v"(v2), "v"(v3), "s"(srd), "s"(lds_dst) : "memory");
}
__device__ __forceinline__ unsigned ld1_dev(u32x4 srd, unsigned voff) {
  unsigned v;
  asm volatile("buffer_load_dword %0, %1, %2, 0 offen sc1\n\ts_waitcnt vmcnt(0)" : "=&v"(v) : "v"(voff), "s"(srd) : "memory");
  return v;
}
__device__ __forceinline__ u32x4 ld4_dev(u32x4 srd, unsigned voff) {
  u32x4 v;
  asm volatile("buffer_load_dwordx4 %0, %1, %2, 0 offen sc1\n\ts_waitcnt vmcnt(0)" : "=&v"(v) : "v"(voff), "s"(srd) : "memory");
  return v;
}
__device__ __forceinline__ void vm_drain() { asm volatile("s_waitcnt vmcnt(0)" ::: "memory"); }
__device__ __forceinline__ void bar_lds() { asm volatile("s_waitcnt lgkmcnt(0)\n\ts_barrier" ::: "memory"); }
template <class T> __device__ __forceinline__ T ldsr(unsigned a) { return *(const LAS T*)a; }
template <class T> __device__ __forceinline__ void ldsw(unsigned a, T v) { *(LAS T*)a = v; }
__device__ __forceinline__ unsigned lds_load(unsigned a) { return __hip_atomic_load((LAS unsigned*)a, __ATOMIC_RELAXED, __HIP_MEMORY_SCOPE_WORKGROUP); }
__device__ __forceinline__ unsigned lds_add_rtn(unsigned a, unsigned v) {
  return __hip_atomic_fetch_add((LAS unsigned*)a, v, __ATOMIC_RELAXED, __HIP_MEMORY_SCOPE_WORKGROUP);
}
__device__ __forceinline__ unsigned lds_add_one(unsigned a, unsigned v) {
  unsigned r;
  asm volatile("s_mov_b64 exec, 1\n\tds_add_rtn_u32 %0, %1, %2\n\ts_mov_b64 exec, -1\n\ts_waitcnt lgkmcnt(0)" : "=&v"(r) : "v"(a), "v"(v) : "memory");
  return __builtin_amdgcn_readfirstlane(r);
}
__device__ __forceinline__ void arrive(unsigned a) {
  asm volatile("s_mov_b64 exec, 1\n\tds_add_u32 %0, %1\n\ts_mov_b64 exec, -1" :: "v"(a), "v"(1u) : "memory");
}
template <int SLEEP = 2>
__device__ __forceinline__ unsigned wait_ge(unsigned a, unsigned n) {
  unsigned v, r, i;
  asm volatile("s_mov_b32 %2, 0x400000\nLW_%=:\n\tds_read_b32 %0, %3\n\ts_waitcnt lgkmcnt(0)\n\tv_readfirstlane_b32 %1, %0\n\ts_cmp_ge_u32 %1, %4\n\t"
               "s_cbranch_scc1 LX_%=\n\ts_sleep %c5\n\ts_sub_u32 %2, %2, 1\n\ts_cbranch_scc0 LW_%=\nLX_%=:"
               : "=&v"(v), "=&s"(r), "=&s"(i) : "v"(a), "i"(n), "i"(SLEEP) : "memory", "scc");
  return r;
}
__device__ __forceinline__ u32x2 tr_read(unsigned a) { return __builtin_bit_cast(u32x2, __builtin_amdgcn_ds_read_tr16_b64_v4i16((LAS s16x4*)a)); }
__device__ __forceinline__ unsigned cvt_pk(float lo, float hi) { return __builtin_bit_cast(unsigned, bf16x2{(__bf16)lo, (__bf16)hi}); }
__device__ __forceinline__ float bf2f(unsigned v) { return __uint_as_float(v << 16); }
__device__ __forceinline__ bf16x2 as_bf2(unsigned v) { return __builtin_bit_cast(bf16x2, v); }
__device__ __forceinline__ f32x16 mfma(u32x4 a, u32x4 b, f32x16 c) {
  return __builtin_amdgcn_mfma_f32_32x32x16_bf16(__builtin_bit_cast(bf16x8, a), __builtin_bit_cast(bf16x8, b), c, 0, 0, 0);
}
__device__ __forceinline__ unsigned half_incl_scan(unsigned v) {
  v += __builtin_amdgcn_update_dpp(0u, v, 0x111, 0xf, 0xf, true);
  v += __builtin_amdgcn_update_dpp(0u, v, 0x112, 0xf, 0xf, true);
  v += __builtin_amdgcn_update_dpp(0u, v, 0x114, 0xf, 0xf, true);
  v += __builtin_amdgcn_update_dpp(0u, v, 0x118, 0xf, 0xf, true);
  v += __builtin_amdgcn_update_dpp(0u, v, 0x142, 0xa, 0xf, true);
  return v;
}
__device__ __forceinline__ unsigned wave_incl_scan(unsigned v) {
  v = half_incl_scan(v);
  return v + __builtin_amdgcn_update_dpp(0u, v, 0x143, 0xc, 0xf, true);
}

template <int NM, bool TR, int SLEEP>
__device__ __forceinline__ void gemm(unsigned ldsW, int rt, int cb, int lane, f32x16 (&c)[NM][2]) {
  const unsigned h = lane >> 5, q = (lane & 15) >> 2, p = lane & 3, row = 32 * rt + (lane & 31);
  const unsigned xa = L_X + (rowbase(row) ^ (16 * h)), ch = 4 * cb + 2 * ((lane >> 4) & 1) + (p >> 1);
  const unsigned wa0 = ldsW + (rowbase(8 * h + q) ^ (16 * ch)) + 8 * (p & 1), wa1 = ldsW + (rowbase(8 * h + 4 + q) ^ (16 * ch)) + 8 * (p & 1);
  const unsigned keep = row + 32 < NB ? ~0u : 0u;
#pragma unroll
  for (int m = 0; m < NM; ++m)
#pragma unroll
    for (int i = 0; i < 16; ++i) { c[m][0][i] = 0.f; c[m][1][i] = 0.f; }
#pragma unroll
  for (int s = 0; s < 8; ++s) {
    const u32x4 a0 = ldsr<u32x4>(xa ^ (32 * s)), a1 = ldsr<u32x4>((xa ^ (32 * s)) + 8192) & keep;
    u32x4 b[NM];
#pragma unroll
    for (int m = 0; m < NM; ++m) {
      const u32x2 b0 = tr_read(wa0 + 4096 * s + 32768 * m), b1 = tr_read(wa1 + 4096 * s + 32768 * m);
      b[m] = u32x4{b0[0], b0[1], b1[0], b1[1]};
    }
#pragma unroll
    for (int m = 0; m < NM; ++m) c[m][0] = TR ? mfma(b[m], a0, c[m][0]) : mfma(a0, b[m], c[m][0]);
#pragma unroll
    for (int m = 0; m < NM; ++m) c[m][1] = TR ? mfma(b[m], a1, c[m][1]) : mfma(a1, b[m], c[m][1]);
    if (s < 7) __builtin_amdgcn_s_sleep(SLEEP);
  }
}
__device__ __forceinline__ void gemm_pack(unsigned ldsW, int rt, int cb, int lane, float scale, u32x2 (&pk)[2][4]) {
  f32x16 c[1][2];
  gemm<1, true, PACE_AU>(ldsW, rt, cb, lane, c);
#pragma unroll
  for (int t = 0; t < 2; ++t)
#pragma unroll
    for (int j = 0; j < 4; ++j) {
      pk[t][j] = u32x2{cvt_pk(c[0][t][4 * j] * scale, c[0][t][4 * j + 1] * scale), cvt_pk(c[0][t][4 * j + 2] * scale, c[0][t][4 * j + 3] * scale)};
      asm volatile("" : "+v"(pk[t][j]));
    }
}

__global__ void __launch_bounds__(1024, 4) gatedgcn(const uint16_t* __restrict__ x, const uint16_t* __restrict__ ea, const int* __restrict__ src,
                                                    const int* __restrict__ dst, const uint16_t* __restrict__ wa, const uint16_t* __restrict__ wb,
                                                    const uint16_t* __restrict__ wc, const uint16_t* __restrict__ wu, const uint16_t* __restrict__ wv_,
                                                    char* __restrict__ ws, uint16_t* __restrict__ out) {
  __shared__ __attribute__((aligned(256))) char smem[L_END];
  if ((unsigned)(size_t)(LAS char*)smem != 0) return;
  const int tid = threadIdx.x, lane = tid & 63, wv = __builtin_amdgcn_readfirstlane(tid >> 6);
  {
    uint64_t code0;
    unsigned codesz;
    asm volatile("s_getpc_b64 %0\nCODE_AT:\n\ts_mov_b32 %1, CODE_END-CODE_AT" : "=s"(code0), "=s"(codesz));
    if (wv < 4 && (wv == 0 || blockIdx.x < 32)) dma4(make_srd((const void*)code0, codesz), tid * 128, L_JUNK);
  }
  asm volatile("" :: "s"(x), "s"(ea), "s"(src), "s"(dst), "s"(wa), "s"(wb), "s"(wc), "s"(wu), "s"(wv_), "s"(ws), "s"(out));
  const int g = 255 - blockIdx.x, bk = (g & 7) * 32 + (g >> 3);
  const int node0 = bk * NB, nn = min(NB, NN - node0), ne = min(CH, NE - g * CH);
  const int h = lane >> 5, c = lane & 31;
  const int role = wv >> 2, gw = wv - 8;
  const u32x4 shand = make_srd(ws, H_END), sseg = make_srd(ws + WS_SEG + (bk >> 2) * 128 + 4 * (bk & 3), 256 * 1024);
  const auto rhand = make_rsrc(ws, H_END);
  if ((wv == 5 || wv == 6) && lane < 8)
    dma4(make_srd(wv == 5 ? wa : wu, 32768), (g >> 3) * 1024 + lane * 128, L_JUNK);
  u32x4 d4[2], s4[2];
  if (role == 0) {
    asm volatile("buffer_load_dwordx4 %0, %4, %6, 0 offen\n\tbuffer_load_dwordx4 %1, %5, %6, 0 offen\n\t"
                 "buffer_load_dwordx4 %2, %4, %7, 0 offen\n\tbuffer_load_dwordx4 %3, %5, %7, 0 offen"
                 : "=&v"(d4[0]), "=&v"(d4[1]), "=&v"(s4[0]), "=&v"(s4[1])
                 : "v"(tid * 16), "v"(tid * 16 + 4096), "s"(make_srd(dst + g * CH, ne * 4)), "s"(make_srd(src + g * CH, ne * 4)) : "memory");
    if (tid < (L_NI - L_CNT) / 16) {
      const unsigned a = L_CNT + 16 * tid, q0 = L_IT + 16;
      ldsw<u32x4>(a, a == S_NEXT ? u32x4{q0, q0 + 4 * QLEN, q0 + 8 * QLEN, q0 + 12 * QLEN} : u32x4{0, 0, 0, 0});
    } else if (wv == 3) ldsw<u32x4>(L1_CNT + 16 * lane, u32x4{0, 0, 0, 0});
  }
  const unsigned sw0 = gw * 4096 + (lane >> 4) * 256 + 16 * ((lane & 15) ^ ((lane >> 4) << 2));
  auto stage_w = [&](const uint16_t* w0, const uint16_t* w1, int o) INL {
    dma64(make_srd(w0 + o / 2, 32768 - o), sw0, sw0 ^ 16, sw0 ^ 32, sw0 ^ 48, L_W + gw * 4096 + o);
    dma64(make_srd(w1 + o / 2, 32768 - o), sw0, sw0 ^ 16, sw0 ^ 32, sw0 ^ 48, L_W + 32768 + gw * 4096 + o);
  };
  if (role == 1) {
    unsigned drop;
    asm volatile(".set PF_AT, 0\n\t.rept 8\n\ts_load_dword %0, %1, PF_AT\n\t.set PF_AT, PF_AT + 8192\n\t.endr\n\ts_barrier\n\ts_waitcnt lgkmcnt(0)"
                 : "=&s"(drop) : "s"(ws + WS_SEG + (bk >> 2) * 128 + (wv - 4) * 65536) : "memory");
  } else bar_lds();
  if (role >= 2) {
    __builtin_amdgcn_s_setprio(2);
    const u32x4 sx = make_srd(x + (size_t)node0 * 128, nn * 256);
    if (gw < 6) dma64(sx, sw0, sw0 ^ 16, sw0 ^ 32, sw0 ^ 48, L_X + gw * 4096);
    else if (gw == 7) dma64(make_srd(wc, 4096), lane * 16, lane * 16, lane * 16, lane * 16, L1_T);
    else if (lane < 32) dma16(sx, sw0, L_X + 6 * 4096);
    stage_w(wb, wv_, 0);
    arrive(S_STAGED);
  } else if (role == 1 && wv < 7) {
    wait_ge<1>(S_STAGED, 8);
    const u32x4 se = make_srd(ea + (size_t)g * CH * 16, ne * 32);
#pragma unroll
    for (int k = 0; k < 4; ++k) { const unsigned o = (wv - 4 + 3 * k) * 4096 + lane * 16; dma64(se, o, o, o, o, L_E + (wv - 4 + 3 * k) * 4096); }
    if (wv == 4) dma16(se, 48 * 1024 + lane * 16, L_E + 48 * 1024);
  }

  if (role >= 2) {
    const int cb = gw >> 1, rt = 2 * ((wv ^ role) & 1);
    vm_drain();
    arrive(S_GEMM); wait_ge(S_GEMM, 8);
    u32x4 wcv = {0, 0, 0, 0};
    if (rt) {
#pragma unroll
      for (int j = 0; j < 4; ++j) {
        const unsigned a = L1_T + (8 * h + 2 * j) * 256 + (32 * cb + c) * 2;
        wcv[j] = cvt_pk(bf2f(ldsr<uint16_t>(a)) * NLOG2E, bf2f(ldsr<uint16_t>(a + 256)) * NLOG2E);
      }
      asm volatile("" : "+v"(wcv));
      arrive(S_TBUF);
    }
    u32x4 tw[8];
    {
      f32x16 bv[2][2];
      if (role == 3) wait_ge(S_GEMM, 12);
      gemm<2, false, PACE_BV>(L_W, rt, cb, lane, bv);
      arrive(S_GEMM);
      const unsigned tb = L1_T + (wv & 3) * 1024;
#pragma unroll
      for (int q = 0; q < 8; ++q) {
        tw[q] = u32x4{0, 0, 0, 0};
        if (q < 5 || rt == 0) {
          unsigned wd[4];
#pragma unroll
          for (int e = 0; e < 4; ++e) {
            const unsigned lo = cvt_pk(bv[0][q >> 2][4 * (q & 3) + e] * NLOG2E, 0.f);
            const int u = __float_as_int(bv[1][q >> 2][4 * (q & 3) + e]);
            const int t = max(__builtin_elementwise_add_sat(u, (int)(0x8000u - lo)), u & (int)0x80000000);
            wd[e] = ((unsigned)t & 0xffff0000u) | lo;
          }
          if (q == 0) { if (role == 2) wait_ge(S_TBUF, 4); else wait_ge(S_TBUF, 8); }
#pragma unroll
          for (int e = 0; e < 4; ++e) ldsw<unsigned>(tb + h * 512 + e * 128 + c * 4, wd[e]);
          asm volatile("" ::: "memory");
          tw[q] = ldsr<u32x4>(tb + lane * 16);
          asm volatile("" ::: "memory");
        }
      }
      arrive(S_TBUF);
#pragma unroll
      for (int q = 0; q < 8; ++q) asm volatile("" : "+v"(tw[q]));
    }
    if (g == 255) {
      const auto rz = make_rsrc(ws + WS_TAB + (size_t)cb * WSTR + (size_t)NN * 128, 64 * 128);
#pragma unroll
      for (int q = 0; q < 4; ++q) __builtin_amdgcn_raw_buffer_store_b128(u32x4{0x7f00u, 0x7f00u, 0x7f00u, 0x7f00u}, rz, rt * 2048 + q * 1024 + lane * 16, 0, DEV);
    }
    wait_ge(S_TAGGED, 1);
    if (role == 2) wait_ge(S_EDONE, 7); else wait_ge<1>(S_LOOK, 4);
    __builtin_amdgcn_s_setprio(3);
    {
      const auto rtab = make_rsrc(ws + WS_TAB + (size_t)cb * WSTR + (size_t)node0 * 128, nn * 128);
#pragma unroll
      for (int q = 0; q < 8; ++q) __builtin_amdgcn_raw_buffer_store_b128(tw[q], rtab, lane * 16 + rt * 4096 + q * 1024, 0, DEV);
    }
    if (role == 2) {
      wait_ge(S_GEMM, 16);
      stage_w(wa, wu, 0);
      stage_w(wa, wu, 16384);
    }
    vm_drain();
    if (role == 2) arrive(S_GEMM);
    if (lds_add_one(S_TDONE, 1) == 7 && lane == 0) __builtin_amdgcn_raw_buffer_store_b32(1u, rhand, H_TAB + 128 * g, 0, DEV);
    wait_ge(S_GEMM, 20);
    u32x2 pa[2][4], pu[2][4];
    gemm_pack(L_W, rt, cb, lane, NLOG2E, pa);
    gemm_pack(L_W + 32768, rt, cb, lane, 1.f, pu);
    arrive(S_AU); wait_ge(S_AU, 8);
#pragma unroll
    for (int t = 0; t < 2; ++t) {
      const unsigned row = 32 * (rt + t) + c, ab = rowbase(row) ^ (64 * cb + 8 * h);
      if (row < NB) {
#pragma unroll
        for (int q = 0; q < 4; ++q) { ldsw<u32x2>(L_A + (ab ^ (16 * q)), pa[t][q]); ldsw<u32x2>(L_U + (ab ^ (16 * q)), pu[t][q]); }
      }
    }
    if (rt) ldsw<u32x4>(L_WC + cb * 1024 + lane * 16, wcv);
    arrive(S_DATA);
  } else {
    if (wv == 7) {
      __builtin_amdgcn_s_setprio(3);
      unsigned old[4];
      if (lane == 0) __builtin_amdgcn_raw_buffer_store_b32(0u, rhand, H_TAB + 128 * g, 0, DEV);
      asm volatile("buffer_load_dword %0, %4, %8, 0 offen sc1\n\tbuffer_load_dword %1, %5, %8, 0 offen sc1\n\t"
                   "buffer_load_dword %2, %6, %8, 0 offen sc1\n\tbuffer_load_dword %3, %7, %8, 0 offen sc1\n\ts_waitcnt vmcnt(0)"
                   : "=&v"(old[0]), "=&v"(old[1]), "=&v"(old[2]), "=&v"(old[3])
                   : "v"(seg_at(lane)), "v"(seg_at(lane + 64)), "v"(seg_at(lane + 128)), "v"(seg_at(lane + 192)), "s"(sseg) : "memory");
#pragma unroll
      for (int k = 0; k < 4; ++k)
        __hip_atomic_fetch_or((LAS unsigned*)(L_BMP + ((old[k] >> 25) & 0x3c)), 1u << ((old[k] >> 22) & 31), __ATOMIC_RELAXED, __HIP_MEMORY_SCOPE_WORKGROUP);
      const unsigned fr = ~lds_load(L_BMP + 4 * (lane & 15));
      const int fw = __builtin_ctzll(__ballot(fr != 0));
      const unsigned tag = 32 * fw + __builtin_ctz(__builtin_amdgcn_readlane(fr, fw));
      if (lane == 0) { __builtin_amdgcn_raw_buffer_store_b32(tag, rhand, H_TAG + 4 * bk, 0, DEV); ldsw<unsigned>(S_TAG, tag); }
      arrive(S_TAGGED);
      wait_ge(S_EOUT, 7);
      const auto rrow = make_rsrc(ws + WS_SEG + seg_at(g), 8192);
      const u32x4 row = ldsr<u32x4>(L1_SEG + 16 * lane);
      u32x4 tg = ld4_dev(shand, H_TAG + 16 * lane);
      wait_ge<0>(S_EDONE, 7);
      __builtin_amdgcn_raw_buffer_store_b128(row | ((tg & 511u) << 22), rrow, 128 * lane, 0, DEV);
#pragma nounroll
      for (int i = 0; i < SPIN; ++i) {
        unsigned seen = 0;
#pragma nounroll
        for (int j = 0; j < (i < 2 ? 1 : 64) && (seen = __builtin_amdgcn_readfirstlane(lds_load(S_SEEN))) < 4; ++j) __builtin_amdgcn_s_sleep(1);
        const u32x4 t2 = ld4_dev(shand, H_TAG + 16 * lane);
        if (t2[0] != tg[0] || t2[1] != tg[1] || t2[2] != tg[2] || t2[3] != tg[3])
          __builtin_amdgcn_raw_buffer_store_b128(row | ((t2 & 511u) << 22), rrow, 128 * lane, 0, DEV);
        tg = t2;
        if (seen >= 4) break;
      }
      wait_ge(S_PAD, 3);
      wait_ge<0>(S_RANKED, 4);
      const u32x2 cn = ldsr<u32x2>(L_CNT + 8 * lane);
      const unsigned slow0 = ldsr<unsigned>(S_SLOW);
      const unsigned k0 = (cn[0] + 1) >> 1, k1 = (cn[1] + 1) >> 1;
      const unsigned b0 = lane < NB / 2 ? 248 - 4 * min(k0, 62u) : 252, b1 = lane < NB / 2 ? 248 - 4 * min(k1, 62u) : 252;
      const unsigned r0 = lds_add_rtn(L_HIST + b0, 4u), r1 = lds_add_rtn(L_HIST + b1, 4u);
      const unsigned hv = lds_load(L_HIST + 4 * lane), hb = wave_incl_scan(hv) - hv + L_SRT;
      ldsw<unsigned>(__builtin_amdgcn_ds_bpermute(b0, hb) + r0, (min(2 * lane, NB) * 8) | (k0 << 10));
      ldsw<unsigned>(__builtin_amdgcn_ds_bpermute(b1, hb) + r1, (min(2 * lane + 1, NB) * 8) | (k1 << 10));
      asm volatile("" ::: "memory");
      u32x4 q = ldsr<u32x4>(L_SRT + 16 * c);
      const unsigned gl = c < NG ? max(max(max(q[0], q[1]), max(q[2], q[3])) >> 10, 1u) : 0;
      const unsigned ginc = half_incl_scan(gl), gb = ginc - gl;
      q &= 1023u;
      {
        const unsigned p = c < NG ? L_EI + gb * 16 + 8 * h : L_EI + 2 * (SCAP - 2), adj = 128 - 128 * h;
        ldsw<u32x2>(L_NI + (c < NG ? (h ? q[2] : q[0]) : 8 * 99), u32x2{p, adj});
        ldsw<u32x2>(L_NI + (h ? q[3] : q[1]), u32x2{p + 2, adj});
      }
      const bool over = slow0 != 0 || __builtin_amdgcn_readlane(ginc, 31) > SCAP / 8 - 1;
      lds_add_one(S_SORTED, over ? 2 : 1);
      if (over) ldsw<unsigned>(S_SLOW, 1u);
      if (lane < NG) {
        q >>= 3;
        const u32x4 lo = {rowbase(q[0]), rowbase(q[1]), rowbase(q[2]), rowbase(q[3])};
        ldsw<u32x2>(L_GN + 8 * lane, u32x2{lo[0] | (lo[1] << 16), lo[2] | (lo[3] << 16)});
        ldsw<u32x4>(L_G2 + 16 * lane, lo | (q << 24));
      }
      if (c < NG && !over) {
#pragma unroll
        for (int j = 0; j < 2; ++j) {
          const int w = 2 * h + j, qu = (c + w) & 3, i = 6 * w + (c >> 2) + (w > qu);
          ldsw<unsigned>(L_IT + 4 * (qu * QLEN + i), gb | (gl << 10) | (w << 20) | (c << 24) | (w << 29));
        }
      }
      arrive(S_GO);
    } else if (role == 0) {
      asm volatile("s_waitcnt vmcnt(0)" : "+v"(d4[0]), "+v"(d4[1]), "+v"(s4[0]), "+v"(s4[1]) :: "memory");
      unsigned bkt[8], rank[8];
#pragma unroll
      for (int j = 0; j < 8; ++j) {
        bkt[j] = d4[j >> 2][j & 3] / NB;
        rank[j] = lds_add_rtn(4 * tid + 1024 * (j >> 2) < ne ? L1_CNT + 4 * bkt[j] : L_JUNK + 4 * lane, 1u);
      }
      arrive(S_BIN); wait_ge(S_BIN, 4);
      const u32x4 a = ldsr<u32x4>(L1_CNT + 16 * lane);
      const unsigned p1 = a[0], p2 = p1 + a[1], p3 = p2 + a[2], tot = p3 + a[3], e = wave_incl_scan(tot) - tot;
      ldsw<u32x4>(L1_SEG + 16 * lane, u32x4{e | (a[0] << 11), (e + p1) | (a[1] << 11), (e + p2) | (a[2] << 11), (e + p3) | (a[3] << 11)});
      asm volatile("" ::: "memory");
#pragma unroll
      for (int j = 0; j < 8; ++j) rank[j] += ldsr<unsigned>(L1_SEG + 4 * bkt[j]) & 0x7ff;
#pragma unroll
      for (int i = 0; i < 2; ++i) {
        if (4 * tid + 1024 * i < ne) {
#pragma unroll
          for (int j = 4 * i; j < 4 * i + 4; ++j) {
            ldsw<uint16_t>(L1_INV + 2 * rank[j], (uint16_t)((4 * tid + 1024 * i + (j & 3)) * 32));
            ldsw<unsigned>(L1_REC + 4 * rank[j], (s4[i][j & 3] << 7) | (d4[i][j & 3] - bkt[j] * NB));
          }
        }
      }
      arrive(S_BIN);
    }
    if (wv < 7) {
      wait_ge(S_BIN, 8);
      unsigned eo[7];
      u32x4 ev[7];
#pragma unroll
      for (int k = 0; k < 7; ++k) eo[k] = ldsr<uint16_t>(L1_INV + 2 * ((wv + 7 * k) * 32 + (lane >> 1))) & 0xffe0u;
      u32x4 rv = ldsr<u32x4>(L1_REC + 16 * tid);
#pragma unroll
      for (int k = 0; k < 7; ++k) { eo[k] += L_E + 16 * (lane & 1); asm volatile("" : "+v"(eo[k])); }
      asm volatile("" : "+v"(rv));
      if (role) { vm_drain(); arrive(S_EIN); }
      wait_ge<1>(S_EIN, 3);
      __builtin_amdgcn_raw_buffer_store_b128(rv, make_rsrc(ws + WS_REC + (size_t)g * CH * 4, ne * 4), 16 * tid, 0, DEV);
      const auto rep = make_rsrc(ws + WS_EP + (size_t)g * CH * 32, ne * 32);
#pragma unroll
      for (int k = 0; k < 7; ++k) ev[k] = ldsr<u32x4>(eo[k]);
#pragma unroll
      for (int k = 0; k < 7; ++k) __builtin_amdgcn_raw_buffer_store_b128(ev[k], rep, (wv + 7 * k) * 1024 + lane * 16, 0, DEV);
      arrive(S_EOUT);
      vm_drain();
      arrive(S_EDONE);
    }
    if (role == 0) {
      __builtin_amdgcn_s_setprio(3);
      wait_ge(S_TAGGED, 1);
      wait_ge(S_EDONE, 7);
      __builtin_amdgcn_s_sleep(FIRST_LOOK);
      const unsigned tag = ldsr<unsigned>(S_TAG);
      unsigned sg = ~0u;
      arrive(S_LOOK);
#pragma nounroll
      for (int i = 0; i < SPIN; ++i) {
        if (sg >> 22 != tag) sg = ld1_dev(sseg, seg_at(tid)) & 0x7fffffffu;
        if (__all(sg >> 22 == tag)) break;
        __builtin_amdgcn_s_sleep(POLL);
      }
      if (sg >> 22 != tag) sg = 0;
      arrive(S_SEEN);
      wait_ge<1>(S_TBUF, 12);
      const unsigned len = (sg >> 11) & 0x7ff, inc = wave_incl_scan(len);
      int etot = __builtin_amdgcn_readlane(inc, 63);
      const int ebase = lds_add_one(S_ETOP, etot);
      if (ebase + etot > ECAP || etot > 512) { ldsw<unsigned>(S_SLOW, 1u); etot = 0; }
      const unsigned eb = L_E + ebase * 32;
      unsigned rec[8], rk[8];
      if (etot) {
        unsigned la = eb + 4 * (inc - len), lv = (tid * CH + (sg & 0x7ff)) * 4, ln = len;
        asm volatile("EX_%=:\n\t"
                     "v_cmpx_lt_u32 vcc, 0, %2\n\tds_write_b32 %0, %1\n\tv_add_u32 %1, 4, %1\n\t"
                     "v_cmpx_lt_u32 vcc, 1, %2\n\tds_write_b32 %0, %1 offset:4\n\tv_add_u32 %1, 4, %1\n\t"
                     "v_cmpx_lt_u32 vcc, 2, %2\n\tds_write_b32 %0, %1 offset:8\n\tv_add_u32 %1, 4, %1\n\t"
                     "v_cmpx_lt_u32 vcc, 3, %2\n\tds_write_b32 %0, %1 offset:12\n\tv_add_u32 %1, 4, %1\n\t"
                     "v_cmpx_lt_u32 vcc, 4, %2\n\tv_add_u32 %0, 16, %0\n\tv_subrev_u32 %2, 4, %2\n\ts_nop 2\n\ts_cbranch_execnz EX_%=\n\t"
                     "s_mov_b64 exec, -1" : "+v"(la), "+v"(lv), "+v"(ln) :: "memory", "vcc");
      }
#pragma unroll
      for (int k = 0; k < 8; ++k) rk[k] = ldsr<unsigned>(eb + 4 * lane + 256 * k);
      ldsw<u32x2>(L_EINF + 8 * wv, u32x2{eb, (unsigned)etot});
      arrive(S_LIST + 4 * wv);
      const auto rrec = make_rsrc(ws + WS_REC, NE * 4);
      const int nv = etot - lane;
#pragma unroll
      for (int k = 0; k < 8; ++k) rec[k] = __builtin_amdgcn_raw_buffer_load_b32(rrec, 64 * k < nv ? rk[k] : 0x80000000u, 0, DEV);
#pragma unroll
      for (int k = 0; k < 8; ++k) rk[k] = lds_add_rtn(L_CNT + (64 * k < nv ? 4 * (rec[k] & 127) : L_JUNK - L_CNT + 4 * lane), 1u);
      arrive(S_RANKED);
      unsigned na[8], ev[8];
#pragma unroll
      for (int k = 0; k < 8; ++k) {
        na[k] = L_NI + (64 * k < nv ? 8 * (rec[k] & 127) : 8 * 99);
        rk[k] = 64 * k < nv ? (rk[k] >> 1) * 16 + (rk[k] & 1) * 4 : 0;
        rec[k] &= ~127u;
        ev[k] = (ebase + lane + 64 * k) * 32;
        asm volatile("" : "+v"(na[k]), "+v"(rk[k]), "+v"(rec[k]), "+v"(ev[k]));
      }
      if (wait_ge<0>(S_SORTED, 1) == 1) {
        u32x2 ni[8];
#pragma unroll
        for (int k = 0; k < 8; ++k) ni[k] = ldsr<u32x2>(na[k]);
#pragma unroll
        for (int k = 0; k < 8; ++k) {
          if (k < 6 || 64 * k < etot) {
            const unsigned p = ni[k][0] + rk[k];
            ldsw<uint16_t>(p, (uint16_t)ev[k]);
            ldsw<unsigned>(2 * p + (L_SRC - 2 * L_EI), rec[k] + ni[k][1]);
          }
        }
      }
      arrive(S_GO);
    } else if (wv < 7) {
      {
        wait_ge(S_EOUT, 7);
        const int t = tid - 256;
        const unsigned dv = (NN + (t & 62) + (g & 1)) * 128 + 128 - 128 * (t & 1), zv = ECAP * 32 * 0x10001u;
#pragma unroll
        for (int k = 0; k < 3; ++k) if (t + 192 * k < SCAP / 4) ldsw<u32x4>(L_SRC + 16 * (t + 192 * k), u32x4{dv, dv, dv, dv});
#pragma unroll
        for (int k = 0; k < 2; ++k) if (t + 192 * k < SCAP / 8) ldsw<u32x4>(L_EI + 16 * (t + 192 * k), u32x4{zv, zv, zv, zv});
        if (t >= 190) ldsw<u32x4>(L_E + ECAP * 32 + 16 * (t - 190), u32x4{0, 0, 0, 0});
        arrive(S_PAD);
      }
      const u32x4 se = make_srd(ws + WS_EP, NE * 32);
      __builtin_amdgcn_s_setprio(2);
#pragma nounroll
      for (int li = wv - 4; li < 4; li += 3) {
        wait_ge<0>(S_LIST + 4 * li, 1);
        const u32x2 ei = ldsr<u32x2>(L_EINF + 8 * li);
        const unsigned eb = __builtin_amdgcn_readfirstlane(ei[0]);
        const int etot = __builtin_amdgcn_readfirstlane(ei[1]);
        unsigned eo[16];
#pragma unroll
        for (int k = 0; k < 16; ++k) eo[k] = ldsr<unsigned>(eb + 4 * (lane >> 1) + 128 * k) * 8 + 16 * (lane & 1);
#pragma unroll
        for (int k = 0; k < 16; ++k) asm volatile("" : "+v"(eo[k]));
#pragma unroll
        for (int k = 0; k < 16; ++k) if (32 * k + (lane >> 1) < etot) dma16_dev(se, eo[k], eb + 1024 * k);
      }
      if (wv > 4) {
        wait_ge(S_SEEN, 4);
        unsigned tf = 0;
#pragma nounroll
        for (int i = 0; i < SPIN; ++i) {
          if (tf != 1u) {
            unsigned t0, t1;
            asm volatile("buffer_load_dword %0, %2, %3, 0 offen sc1\n\tbuffer_load_dword %1, %2, %3, 0 offen offset:128 sc1\n\ts_waitcnt vmcnt(0)"
                         : "=&v"(t0), "=&v"(t1) : "v"(H_TAB + 256 * (tid - 320)), "s"(shand) : "memory");
            tf = t0 & t1;
          }
          if (i == 0) arrive(S_DATA);
          if (__all(tf == 1u)) break;
          __builtin_amdgcn_s_sleep(POLL);
        }
        arrive(S_GO);
      } else {
        arrive(S_GO);
        vm_drain();
        arrive(S_DATA);
      }
    }
  }

  const uint64_t tabp = (uint64_t)(ws + WS_TAB - 128);
  const u32x4 so = make_srd(out + (size_t)node0 * 128, nn * 256);
  u32x8 srds = {(unsigned)tabp, ((unsigned)(tabp >> 32) & 0xffffu) | (4u << 16), 4u * WSTR + 128u, 1u << 23, so[0], so[1], so[2], so[3]};
  unsigned ln = threadIdx.x;
  asm volatile("" : "+v"(ln));
  const unsigned lh = (ln >> 5) & 1, lc = ln & 31, off1 = (ln & 63) ? 0x8000u : 0u;
  asm volatile(
      "s_mov_b32 s51, s40\n\t"
      "s_mov_b32 s55, s41\n\t"
      "s_getpc_b64 s[62:63]\n\t"
      "PC_%=:\n\t"
      "s_mov_b32 s66, P1_%=-PC_%=\n\t"
      "s_mov_b32 s67, P1_%=-PC_%=\n\t"
      "s_mov_b32 s54, 0x3f80\n\t"
      "s_mov_b32 s74, 0x1fffffff\n\t"
      "s_setprio 1\n\t"
      "v_mov_b32_e32 v27, 0\n\t"
      "s_mov_b32 s68, 0x4e800000\n\t"
      "s_mov_b32 s70, 0xc2700000\n\t"
      "s_mov_b32 s72, %c[eps]\n\t"
      "s_mov_b32 s73, %c[eps]\n\t"
      "v_mov_b32_e32 v40, 0x30800000\n\t"
      "v_mov_b32_e32 v24, 4\n\t"
      "v_mov_b32_e32 v32, %[c2]\n\t"
      "v_mov_b64_e32 v[42:43], s[72:73]\n\t"
      "v_mov_b64_e32 v[44:45], 0\n\t"
      "s_mov_b32 s53, 0x400000\n\t"
      "LW_%=:\n\t"
      "ds_read_b32 v26, %[gcb] offset:104\n\t"
      "ds_read_b32 v30, %[it0]\n\t"
      "s_sub_u32 s53, s53, 1\n\t"
      "s_waitcnt lgkmcnt(0)\n\t"
      "s_cbranch_scc1 END_%=\n\t"
      "v_readfirstlane_b32 s52, v26\n\t"
      "s_cmp_ge_u32 s52, 8\n\t"
      "s_cbranch_scc1 LX_%=\n\t"
      "s_sleep 1\n\t"
      "s_branch LW_%=\n\t"
      "LX_%=:\n\t"
      "v_readfirstlane_b32 s50, v30\n\t"
      "s_and_b32 s48, s50, 0x3ff\n\t"
      "s_bfe_u32 s49, s50, 0xa000a\n\t"
      "s_bfe_u32 s52, s50, 0x20014\n\t"
      "s_mul_i32 s52, s52, %c[wstr]\n\t"
      "s_add_u32 s40, s51, s52\n\t"
      "s_addc_u32 s41, s55, 0\n\t"
      "s_cmp_eq_u32 s49, 0\n\t"
      "s_cbranch_scc1 END_%=\n\t"
      "v_lshl_add_u32 v26, s48, 5, %[qb]\n\t"
      "v_lshl_add_u32 v25, s48, 4, %[eib]\n\t"
      "ds_read_b128 v[64:67], v26 offset:0\n\t"
      "ds_read_b128 v[68:71], v26 offset:32\n\t"
      "ds_read_b128 v[72:75], v26 offset:64\n\t"
      "ds_read_b128 v[76:79], v26 offset:96\n\t"
      "ds_read_u16 v31, v25\n\t"
      "s_cmp_le_u32 s49, 4\n\t"
      "s_waitcnt lgkmcnt(0)\n\t"
      "s_cbranch_scc0 T010_%=\n\t"
      "ds_add_rtn_u32 v30, %[nxa], v24\n\t"
      "s_waitcnt lgkmcnt(0)\n\t"
      "ds_read_b32 v30, v30\n\t"
      "s_branch T011_%=\n\t"
      "P1_%=:\n\t"
      "s_bfe_u32 s52, s56, 0x50018\n\t"
      "v_lshl_add_u32 v25, s52, 3, %[gnb]\n\t"
      "s_mov_b32 s53, 0x400000\n\t"
      "PW_%=:\n\t"
      "ds_read_b32 v26, %[gcb] offset:100\n\t"
      "ds_read_b32 v29, v25\n\t"
      "s_sub_u32 s53, s53, 1\n\t"
      "s_waitcnt lgkmcnt(0)\n\t"
      "s_cbranch_scc1 END_%=\n\t"
      "v_readfirstlane_b32 s52, v26\n\t"
      "s_cmp_ge_u32 s52, 11\n\t"
      "s_cbranch_scc1 PX_%=\n\t"
      "s_sleep 1\n\t"
      "s_branch PW_%=\n\t"
      "PX_%=:\n\t"
      "s_setprio 3\n\t"
      "v_xor_b32_sdwa v34, v29, %[c2] dst_sel:DWORD dst_unused:UNUSED_PAD src0_sel:WORD_0 src1_sel:DWORD\n\t"
      "v_xor_b32_sdwa v35, v29, %[c2] dst_sel:DWORD dst_unused:UNUSED_PAD src0_sel:WORD_1 src1_sel:DWORD\n\t"
      "v_add_u32_e32 v26, v31, %[eb]\n\t"
      "ds_read_u16_d16_hi v46, v34 offset:%c[ao]\n\t"
      "ds_read_u16_d16_hi v47, v35 offset:%c[ao]\n\t"
      "ds_read_b128 v[56:59], %[wfb]\n\t"
      "ds_read_b128 v[60:63], v26\n\t"
      "v_lshl_add_u32 v26, s48, 5, %[qb]\n\t"
      "v_lshl_add_u32 v25, s48, 4, %[eib]\n\t"
      "ds_read_b128 v[64:67], v26 offset:0\n\t"
      "ds_read_b128 v[68:71], v26 offset:32\n\t"
      "ds_read_b128 v[72:75], v26 offset:64\n\t"
      "ds_read_b128 v[76:79], v26 offset:96\n\t"
      "ds_read_u16 v31, v25\n\t"
      "s_cmp_le_u32 s49, 4\n\t"
      "s_waitcnt lgkmcnt(0)\n\t"
      "s_cbranch_scc0 T110_%=\n\t"
      "ds_add_rtn_u32 v30, %[nxa], v24\n\t"
      "s_waitcnt lgkmcnt(0)\n\t"
      "ds_read_b32 v30, v30\n\t"
      "s_branch T111_%=\n\t"
      ".p2align 6\n\t"
      "B000_%=:\n\t"
      "v_dot2_f32_bf16 v80, v96, s54, v46\n\t"
      "v_dot2_f32_bf16 v81, v97, s54, v47\n\t"
      "v_dot2_f32_bf16 v82, v98, s54, v46\n\t"
      "v_dot2_f32_bf16 v83, v99, s54, v47\n\t"
      "v_dot2_f32_bf16 v84, v100, s54, v46\n\t"
      "v_dot2_f32_bf16 v85, v101, s54, v47\n\t"
      "v_dot2_f32_bf16 v86, v102, s54, v46\n\t"
      "v_dot2_f32_bf16 v87, v103, s54, v47\n\t"
      "v_dot2_f32_bf16 v88, v104, s54, v46\n\t"
      "v_dot2_f32_bf16 v89, v105, s54, v47\n\t"
      "v_dot2_f32_bf16 v90, v106, s54, v46\n\t"
      "v_dot2_f32_bf16 v91, v107, s54, v47\n\t"
      "v_dot2_f32_bf16 v92, v108, s54, v46\n\t"
      "v_dot2_f32_bf16 v93, v109, s54, v47\n\t"
      "v_dot2_f32_bf16 v94, v110, s54, v46\n\t"
      "v_dot2_f32_bf16 v95, v111, s54, v47\n\t"
      "DD000_%=:\n\t"
      "v_add_u32_e32 v26, v31, %[eb]\n\t"
      "s_nop 1\n\t"
      "v_mfma_f32_32x32x16_bf16 v[80:95], v[60:63], v[56:59], v[80:95]\n\t"
      "ds_read_b128 v[60:63], v26\n\t"
      "v_lshl_add_u32 v26, s48, 5, %[qb]\n\t"
      "v_lshl_add_u32 v25, s48, 4, %[eib]\n\t"
      "ds_read_b128 v[64:67], v26 offset:0\n\t"
      "ds_read_b128 v[68:71], v26 offset:32\n\t"
      "ds_read_b128 v[72:75], v26 offset:64\n\t"
      "ds_read_b128 v[76:79], v26 offset:96\n\t"
      "ds_read_u16 v31, v25\n\t"
      "s_add_u32 s48, s48, 4\n\t"
      "s_sub_u32 s49, s49, 4\n\t"
      "s_mov_b32 s56, s50\n\t"
      "s_mov_b32 s64, B000_%=-PC_%=\n\t"
      "s_mov_b32 s65, B001_%=-PC_%=\n\t"
      "v_exp_f32_e64 v80, v80 clamp\n\t"
      "v_exp_f32_e64 v81, v81 clamp\n\t"
      "v_exp_f32_e64 v82, v82 clamp\n\t"
      "v_exp_f32_e64 v83, v83 clamp\n\t"
      "v_exp_f32_e64 v84, v84 clamp\n\t"
      "v_exp_f32_e64 v85, v85 clamp\n\t"
      "v_exp_f32_e64 v86, v86 clamp\n\t"
      "v_exp_f32_e64 v87, v87 clamp\n\t"
      "v_pk_fma_f32 v[80:81], v[80:81], s[68:69], v[40:41] op_sel_hi:[1,0,0]\n\t"
      "v_pk_fma_f32 v[82:83], v[82:83], s[68:69], v[40:41] op_sel_hi:[1,0,0]\n\t"
      "v_pk_fma_f32 v[84:85], v[84:85], s[68:69], v[40:41] op_sel_hi:[1,0,0]\n\t"
      "v_pk_fma_f32 v[86:87], v[86:87], s[68:69], v[40:41] op_sel_hi:[1,0,0]\n\t"
      "v_pk_mul_f32 v[48:49], v[80:81], v[82:83]\n\t"
      "v_pk_mul_f32 v[50:51], v[84:85], v[86:87]\n\t"
      "v_pk_mul_f32 v[98:99], v[98:99], v[80:81]\n\t"
      "v_pk_mul_f32 v[52:53], v[48:49], v[50:51]\n\t"
      "v_pk_mul_f32 v[102:103], v[102:103], v[84:85]\n\t"
      "v_rcp_f32_e32 v52, v52\n\t"
      "v_rcp_f32_e32 v53, v53\n\t"
      "v_pk_fma_f32 v[54:55], v[96:97], v[82:83], v[98:99]\n\t"
      "v_pk_add_f32 v[80:81], v[80:81], v[82:83]\n\t"
      "v_pk_fma_f32 v[82:83], v[100:101], v[86:87], v[102:103]\n\t"
      "v_pk_mul_f32 v[50:51], v[50:51], v[52:53]\n\t"
      "v_pk_mul_f32 v[48:49], v[48:49], v[52:53]\n\t"
      "v_pk_add_f32 v[84:85], v[84:85], v[86:87]\n\t"
      "v_pk_fma_f32 v[44:45], v[54:55], v[50:51], v[44:45]\n\t"
      "v_pk_fma_f32 v[42:43], v[80:81], v[50:51], v[42:43]\n\t"
      "v_pk_fma_f32 v[44:45], v[82:83], v[48:49], v[44:45]\n\t"
      "v_pk_fma_f32 v[42:43], v[84:85], v[48:49], v[42:43]\n\t"
      "v_exp_f32_e64 v88, v88 clamp\n\t"
      "v_exp_f32_e64 v89, v89 clamp\n\t"
      "v_exp_f32_e64 v90, v90 clamp\n\t"
      "v_exp_f32_e64 v91, v91 clamp\n\t"
      "v_exp_f32_e64 v92, v92 clamp\n\t"
      "v_exp_f32_e64 v93, v93 clamp\n\t"
      "v_exp_f32_e64 v94, v94 clamp\n\t"
      "v_exp_f32_e64 v95, v95 clamp\n\t"
      "v_pk_fma_f32 v[88:89], v[88:89], s[68:69], v[40:41] op_sel_hi:[1,0,0]\n\t"
      "v_pk_fma_f32 v[90:91], v[90:91], s[68:69], v[40:41] op_sel_hi:[1,0,0]\n\t"
      "v_pk_fma_f32 v[92:93], v[92:93], s[68:69], v[40:41] op_sel_hi:[1,0,0]\n\t"
      "v_pk_fma_f32 v[94:95], v[94:95], s[68:69], v[40:41] op_sel_hi:[1,0,0]\n\t"
      "v_pk_mul_f32 v[48:49], v[88:89], v[90:91]\n\t"
      "v_pk_mul_f32 v[50:51], v[92:93], v[94:95]\n\t"
      "v_pk_mul_f32 v[106:107], v[106:107], v[88:89]\n\t"
      "v_pk_mul_f32 v[52:53], v[48:49], v[50:51]\n\t"
      "v_pk_mul_f32 v[110:111], v[110:111], v[92:93]\n\t"
      "v_rcp_f32_e32 v52, v52\n\t"
      "v_rcp_f32_e32 v53, v53\n\t"
      "v_pk_fma_f32 v[54:55], v[104:105], v[90:91], v[106:107]\n\t"
      "v_pk_add_f32 v[88:89], v[88:89], v[90:91]\n\t"
      "v_pk_fma_f32 v[90:91], v[108:109], v[94:95], v[110:111]\n\t"
      "v_pk_mul_f32 v[50:51], v[50:51], v[52:53]\n\t"
      "v_pk_mul_f32 v[48:49], v[48:49], v[52:53]\n\t"
      "v_pk_add_f32 v[92:93], v[92:93], v[94:95]\n\t"
      "v_pk_fma_f32 v[44:45], v[54:55], v[50:51], v[44:45]\n\t"
      "v_pk_fma_f32 v[42:43], v[88:89], v[50:51], v[42:43]\n\t"
      "v_pk_fma_f32 v[44:45], v[90:91], v[48:49], v[44:45]\n\t"
      "v_pk_fma_f32 v[42:43], v[92:93], v[48:49], v[42:43]\n\t"
      "T000_%=:\n\t"
      "s_waitcnt vmcnt(0) lgkmcnt(0)\n\t"
      "buffer_load_dword v96, v64, s[40:43], 0 offen sc1\n\t"
      "buffer_load_dword v97, v65, s[40:43], 0 offen sc1\n\t"
      "buffer_load_dword v98, v66, s[40:43], 0 offen sc1\n\t"
      "buffer_load_dword v99, v67, s[40:43], 0 offen sc1\n\t"
      "buffer_load_dword v100, v68, s[40:43], 0 offen sc1\n\t"
      "buffer_load_dword v101, v69, s[40:43], 0 offen sc1\n\t"
      "buffer_load_dword v102, v70, s[40:43], 0 offen sc1\n\t"
      "buffer_load_dword v103, v71, s[40:43], 0 offen sc1\n\t"
      "buffer_load_dword v104, v72, s[40:43], 0 offen sc1\n\t"
      "buffer_load_dword v105, v73, s[40:43], 0 offen sc1\n\t"
      "buffer_load_dword v106, v74, s[40:43], 0 offen sc1\n\t"
      "buffer_load_dword v107, v75, s[40:43], 0 offen sc1\n\t"
      "buffer_load_dword v108, v76, s[40:43], 0 offen sc1\n\t"
      "buffer_load_dword v109, v77, s[40:43], 0 offen sc1\n\t"
      "buffer_load_dword v110, v78, s[40:43], 0 offen sc1\n\t"
      "buffer_load_dword v111, v79, s[40:43], 0 offen sc1\n\t"
      "s_cmp_le_u32 s49, 4\n\t"
      "s_cselect_b32 s60, s67, s66\n\t"
      "s_add_u32 s60, s60, s62\n\t"
      "s_addc_u32 s61, s63, 0\n\t"
      "s_setpc_b64 s[60:61]\n\t"
      ".p2align 6\n\t"
      "B001_%=:\n\t"
      "v_dot2_f32_bf16 v80, v96, s54, v46\n\t"
      "v_dot2_f32_bf16 v81, v97, s54, v47\n\t"
      "v_dot2_f32_bf16 v82, v98, s54, v46\n\t"
      "v_dot2_f32_bf16 v83, v99, s54, v47\n\t"
      "v_dot2_f32_bf16 v84, v100, s54, v46\n\t"
      "v_dot2_f32_bf16 v85, v101, s54, v47\n\t"
      "v_dot2_f32_bf16 v86, v102, s54, v46\n\t"
      "v_dot2_f32_bf16 v87, v103, s54, v47\n\t"
      "v_dot2_f32_bf16 v88, v104, s54, v46\n\t"
      "v_dot2_f32_bf16 v89, v105, s54, v47\n\t"
      "v_dot2_f32_bf16 v90, v106, s54, v46\n\t"
      "v_dot2_f32_bf16 v91, v107, s54, v47\n\t"
      "v_dot2_f32_bf16 v92, v108, s54, v46\n\t"
      "v_dot2_f32_bf16 v93, v109, s54, v47\n\t"
      "v_dot2_f32_bf16 v94, v110, s54, v46\n\t"
      "v_dot2_f32_bf16 v95, v111, s54, v47\n\t"
      "DD001_%=:\n\t"
      "v_add_u32_e32 v26, v31, %[eb]\n\t"
      "s_nop 1\n\t"
      "v_mfma_f32_32x32x16_bf16 v[80:95], v[60:63], v[56:59], v[80:95]\n\t"
      "ds_read_b128 v[60:63], v26\n\t"
      "ds_add_rtn_u32 v30, %[nxa], v24\n\t"
      "v_lshl_add_u32 v26, s48, 5, %[qb]\n\t"
      "v_lshl_add_u32 v25, s48, 4, %[eib]\n\t"
      "ds_read_b128 v[64:67], v26 offset:0\n\t"
      "ds_read_b128 v[68:71], v26 offset:32\n\t"
      "ds_read_b128 v[72:75], v26 offset:64\n\t"
      "ds_read_b128 v[76:79], v26 offset:96\n\t"
      "ds_read_u16 v31, v25\n\t"
      "s_mov_b32 s56, s50\n\t"
      "s_mov_b32 s58, s49\n\t"
      "s_mov_b32 s64, B010_%=-PC_%=\n\t"
      "s_mov_b32 s65, B011_%=-PC_%=\n\t"
      "v_exp_f32_e64 v80, v80 clamp\n\t"
      "v_exp_f32_e64 v81, v81 clamp\n\t"
      "v_exp_f32_e64 v82, v82 clamp\n\t"
      "v_exp_f32_e64 v83, v83 clamp\n\t"
      "v_exp_f32_e64 v84, v84 clamp\n\t"
      "v_exp_f32_e64 v85, v85 clamp\n\t"
      "v_exp_f32_e64 v86, v86 clamp\n\t"
      "v_exp_f32_e64 v87, v87 clamp\n\t"
      "v_pk_fma_f32 v[80:81], v[80:81], s[68:69], v[40:41] op_sel_hi:[1,0,0]\n\t"
      "v_pk_fma_f32 v[82:83], v[82:83], s[68:69], v[40:41] op_sel_hi:[1,0,0]\n\t"
      "v_pk_fma_f32 v[84:85], v[84:85], s[68:69], v[40:41] op_sel_hi:[1,0,0]\n\t"
      "v_pk_fma_f32 v[86:87], v[86:87], s[68:69], v[40:41] op_sel_hi:[1,0,0]\n\t"
      "v_pk_mul_f32 v[48:49], v[80:81], v[82:83]\n\t"
      "v_pk_mul_f32 v[50:51], v[84:85], v[86:87]\n\t"
      "v_pk_mul_f32 v[98:99], v[98:99], v[80:81]\n\t"
      "v_pk_mul_f32 v[52:53], v[48:49], v[50:51]\n\t"
      "v_pk_mul_f32 v[102:103], v[102:103], v[84:85]\n\t"
      "v_rcp_f32_e32 v52, v52\n\t"
      "v_rcp_f32_e32 v53, v53\n\t"
      "v_pk_fma_f32 v[54:55], v[96:97], v[82:83], v[98:99]\n\t"
      "v_pk_add_f32 v[80:81], v[80:81], v[82:83]\n\t"
      "v_pk_fma_f32 v[82:83], v[100:101], v[86:87], v[102:103]\n\t"
      "v_pk_mul_f32 v[50:51], v[50:51], v[52:53]\n\t"
      "v_pk_mul_f32 v[48:49], v[48:49], v[52:53]\n\t"
      "v_pk_add_f32 v[84:85], v[84:85], v[86:87]\n\t"
      "v_pk_fma_f32 v[44:45], v[54:55], v[50:51], v[44:45]\n\t"
      "v_pk_fma_f32 v[42:43], v[80:81], v[50:51], v[42:43]\n\t"
      "v_pk_fma_f32 v[44:45], v[82:83], v[48:49], v[44:45]\n\t"
      "v_pk_fma_f32 v[42:43], v[84:85], v[48:49], v[42:43]\n\t"
      "s_waitcnt lgkmcnt(0)\n\t"
      "ds_read_b32 v30, v30\n\t"
      "v_exp_f32_e64 v88, v88 clamp\n\t"
      "v_exp_f32_e64 v89, v89 clamp\n\t"
      "v_exp_f32_e64 v90, v90 clamp\n\t"
      "v_exp_f32_e64 v91, v91 clamp\n\t"
      "v_exp_f32_e64 v92, v92 clamp\n\t"
      "v_exp_f32_e64 v93, v93 clamp\n\t"
      "v_exp_f32_e64 v94, v94 clamp\n\t"
      "v_exp_f32_e64 v95, v95 clamp\n\t"
      "v_pk_fma_f32 v[88:89], v[88:89], s[68:69], v[40:41] op_sel_hi:[1,0,0]\n\t"
      "v_pk_fma_f32 v[90:91], v[90:91], s[68:69], v[40:41] op_sel_hi:[1,0,0]\n\t"
      "v_pk_fma_f32 v[92:93], v[92:93], s[68:69], v[40:41] op_sel_hi:[1,0,0]\n\t"
      "v_pk_fma_f32 v[94:95], v[94:95], s[68:69], v[40:41] op_sel_hi:[1,0,0]\n\t"
      "v_pk_mul_f32 v[48:49], v[88:89], v[90:91]\n\t"
      "v_pk_mul_f32 v[50:51], v[92:93], v[94:95]\n\t"
      "v_pk_mul_f32 v[106:107], v[106:107], v[88:89]\n\t"
      "v_pk_mul_f32 v[52:53], v[48:49], v[50:51]\n\t"
      "v_pk_mul_f32 v[110:111], v[110:111], v[92:93]\n\t"
      "v_rcp_f32_e32 v52, v52\n\t"
      "v_rcp_f32_e32 v53, v53\n\t"
      "v_pk_fma_f32 v[54:55], v[104:105], v[90:91], v[106:107]\n\t"
      "v_pk_add_f32 v[88:89], v[88:89], v[90:91]\n\t"
      "v_pk_fma_f32 v[90:91], v[108:109], v[94:95], v[110:111]\n\t"
      "v_pk_mul_f32 v[50:51], v[50:51], v[52:53]\n\t"
      "v_pk_mul_f32 v[48:49], v[48:49], v[52:53]\n\t"
      "v_pk_add_f32 v[92:93], v[92:93], v[94:95]\n\t"
      "v_pk_fma_f32 v[44:45], v[54:55], v[50:51], v[44:45]\n\t"
      "v_pk_fma_f32 v[42:43], v[88:89], v[50:51], v[42:43]\n\t"
      "v_pk_fma_f32 v[44:45], v[90:91], v[48:49], v[44:45]\n\t"
      "v_pk_fma_f32 v[42:43], v[92:93], v[48:49], v[42:43]\n\t"
      "T001_%=:\n\t"
      "s_cmp_eq_u32 s49, 0\n\t"
      "s_cbranch_scc1 X0001_%=\n\t"
      "s_waitcnt vmcnt(0)\n\t"
      "buffer_load_dword v96, v64, s[40:43], 0 offen sc1\n\t"
      "buffer_load_dword v97, v65, s[40:43], 0 offen sc1\n\t"
      "buffer_load_dword v98, v66, s[40:43], 0 offen sc1\n\t"
      "buffer_load_dword v99, v67, s[40:43], 0 offen sc1\n\t"
      "s_cmp_eq_u32 s49, 1\n\t"
      "s_cbranch_scc1 XO001_%=\n\t"
      "buffer_load_dword v100, v68, s[40:43], 0 offen sc1\n\t"
      "buffer_load_dword v101, v69, s[40:43], 0 offen sc1\n\t"
      "buffer_load_dword v102, v70, s[40:43], 0 offen sc1\n\t"
      "buffer_load_dword v103, v71, s[40:43], 0 offen sc1\n\t"
      "s_cmp_eq_u32 s49, 2\n\t"
      "s_cbranch_scc1 XO001_%=\n\t"
      "buffer_load_dword v104, v72, s[40:43], 0 offen sc1\n\t"
      "buffer_load_dword v105, v73, s[40:43], 0 offen sc1\n\t"
      "buffer_load_dword v106, v74, s[40:43], 0 offen sc1\n\t"
      "buffer_load_dword v107, v75, s[40:43], 0 offen sc1\n\t"
      "s_cmp_eq_u32 s49, 3\n\t"
      "s_cbranch_scc1 XO001_%=\n\t"
      "buffer_load_dword v108, v76, s[40:43], 0 offen sc1\n\t"
      "buffer_load_dword v109, v77, s[40:43], 0 offen sc1\n\t"
      "buffer_load_dword v110, v78, s[40:43], 0 offen sc1\n\t"
      "buffer_load_dword v111, v79, s[40:43], 0 offen sc1\n\t"
      "XO001_%=:\n\t"
      "s_waitcnt lgkmcnt(0)\n\t"
      "v_readfirstlane_b32 s50, v30\n\t"
      "s_and_b32 s48, s50, 0x3ff\n\t"
      "s_bfe_u32 s49, s50, 0xa000a\n\t"
      "s_bfe_u32 s52, s50, 0x20014\n\t"
      "s_mul_i32 s52, s52, %c[wstr]\n\t"
      "s_add_u32 s40, s51, s52\n\t"
      "s_addc_u32 s41, s55, 0\n\t"
      "s_cmp_le_u32 s49, 4\n\t"
      "s_cselect_b32 s60, s67, s66\n\t"
      "s_add_u32 s60, s60, s62\n\t"
      "s_addc_u32 s61, s63, 0\n\t"
      "s_setpc_b64 s[60:61]\n\t"
      "X0001_%=:\n\t"
      "s_mov_b32 s64, END_%=-PC_%=\n\t"
      "s_mov_b32 s65, END_%=-PC_%=\n\t"
      "s_cmp_eq_u32 s64, s66\n\t"
      "s_cbranch_scc1 XO001_%=\n\t"
      "s_waitcnt vmcnt(0)\n\t"
      "s_branch XO001_%=\n\t"
      ".p2align 6\n\t"
      "B010_%=:\n\t"
      "v_dot2_f32_bf16 v80, v96, s54, v46\n\t"
      "v_dot2_f32_bf16 v81, v97, s54, v47\n\t"
      "v_dot2_f32_bf16 v82, v98, s54, v46\n\t"
      "v_dot2_f32_bf16 v83, v99, s54, v47\n\t"
      "s_cmp_eq_u32 s58, 1\n\t"
      "s_cbranch_scc1 DD010_%=\n\t"
      "v_dot2_f32_bf16 v84, v100, s54, v46\n\t"
      "v_dot2_f32_bf16 v85, v101, s54, v47\n\t"
      "v_dot2_f32_bf16 v86, v102, s54, v46\n\t"
      "v_dot2_f32_bf16 v87, v103, s54, v47\n\t"
      "s_cmp_eq_u32 s58, 2\n\t"
      "s_cbranch_scc1 DD010_%=\n\t"
      "v_dot2_f32_bf16 v88, v104, s54, v46\n\t"
      "v_dot2_f32_bf16 v89, v105, s54, v47\n\t"
      "v_dot2_f32_bf16 v90, v106, s54, v46\n\t"
      "v_dot2_f32_bf16 v91, v107, s54, v47\n\t"
      "s_cmp_eq_u32 s58, 3\n\t"
      "s_cbranch_scc1 DD010_%=\n\t"
      "v_dot2_f32_bf16 v92, v108, s54, v46\n\t"
      "v_dot2_f32_bf16 v93, v109, s54, v47\n\t"
      "v_dot2_f32_bf16 v94, v110, s54, v46\n\t"
      "v_dot2_f32_bf16 v95, v111, s54, v47\n\t"
      "DD010_%=:\n\t"
      "v_add_u32_e32 v26, v31, %[eb]\n\t"
      "s_nop 1\n\t"
      "v_mfma_f32_32x32x16_bf16 v[80:95], v[60:63], v[56:59], v[80:95]\n\t"
      "ds_read_b128 v[60:63], v26\n\t"
      "s_bfe_u32 s52, s57, 0x50018\n\t"
      "v_lshl_add_u32 v26, s52, 3, %[gnb]\n\t"
      "ds_read_b32 v29, v26\n\t"
      "v_lshl_add_u32 v26, s48, 5, %[qb]\n\t"
      "v_lshl_add_u32 v25, s48, 4, %[eib]\n\t"
      "ds_read_b128 v[64:67], v26 offset:0\n\t"
      "ds_read_b128 v[68:71], v26 offset:32\n\t"
      "ds_read_b128 v[72:75], v26 offset:64\n\t"
      "ds_read_b128 v[76:79], v26 offset:96\n\t"
      "ds_read_u16 v31, v25\n\t"
      "ds_read_u16_d16_hi v38, v34 offset:%c[uo]\n\t"
      "ds_read_u16_d16_hi v39, v35 offset:%c[uo]\n\t"
      "ds_read_u16_d16_hi v36, v34\n\t"
      "ds_read_u16_d16_hi v37, v35\n\t"
      "v_exp_f32_e64 v80, v80 clamp\n\t"
      "v_exp_f32_e64 v81, v81 clamp\n\t"
      "v_exp_f32_e64 v82, v82 clamp\n\t"
      "v_exp_f32_e64 v83, v83 clamp\n\t"
      "v_pk_fma_f32 v[80:81], v[80:81], s[68:69], v[40:41] op_sel_hi:[1,0,0]\n\t"
      "v_pk_fma_f32 v[82:83], v[82:83], s[68:69], v[40:41] op_sel_hi:[1,0,0]\n\t"
      "v_pk_mul_f32 v[50:51], v[98:99], v[80:81]\n\t"
      "v_pk_mul_f32 v[48:49], v[80:81], v[82:83]\n\t"
      "v_pk_add_f32 v[52:53], v[80:81], v[82:83]\n\t"
      "v_rcp_f32_e32 v48, v48\n\t"
      "v_rcp_f32_e32 v49, v49\n\t"
      "v_pk_fma_f32 v[50:51], v[96:97], v[82:83], v[50:51]\n\t"
      "v_pk_fma_f32 v[42:43], v[52:53], v[48:49], v[42:43]\n\t"
      "v_pk_fma_f32 v[44:45], v[50:51], v[48:49], v[44:45]\n\t"
      "s_cmp_eq_u32 s58, 1\n\t"
      "s_cbranch_scc1 SD010_%=\n\t"
      "v_exp_f32_e64 v84, v84 clamp\n\t"
      "v_exp_f32_e64 v85, v85 clamp\n\t"
      "v_exp_f32_e64 v86, v86 clamp\n\t"
      "v_exp_f32_e64 v87, v87 clamp\n\t"
      "v_pk_fma_f32 v[84:85], v[84:85], s[68:69], v[40:41] op_sel_hi:[1,0,0]\n\t"
      "v_pk_fma_f32 v[86:87], v[86:87], s[68:69], v[40:41] op_sel_hi:[1,0,0]\n\t"
      "v_pk_mul_f32 v[50:51], v[102:103], v[84:85]\n\t"
      "v_pk_mul_f32 v[48:49], v[84:85], v[86:87]\n\t"
      "v_pk_add_f32 v[52:53], v[84:85], v[86:87]\n\t"
      "v_rcp_f32_e32 v48, v48\n\t"
      "v_rcp_f32_e32 v49, v49\n\t"
      "v_pk_fma_f32 v[50:51], v[100:101], v[86:87], v[50:51]\n\t"
      "v_pk_fma_f32 v[42:43], v[52:53], v[48:49], v[42:43]\n\t"
      "v_pk_fma_f32 v[44:45], v[50:51], v[48:49], v[44:45]\n\t"
      "s_cmp_eq_u32 s58, 2\n\t"
      "s_cbranch_scc1 SD010_%=\n\t"
      "v_exp_f32_e64 v88, v88 clamp\n\t"
      "v_exp_f32_e64 v89, v89 clamp\n\t"
      "v_exp_f32_e64 v90, v90 clamp\n\t"
      "v_exp_f32_e64 v91, v91 clamp\n\t"
      "v_pk_fma_f32 v[88:89], v[88:89], s[68:69], v[40:41] op_sel_hi:[1,0,0]\n\t"
      "v_pk_fma_f32 v[90:91], v[90:91], s[68:69], v[40:41] op_sel_hi:[1,0,0]\n\t"
      "v_pk_mul_f32 v[50:51], v[106:107], v[88:89]\n\t"
      "v_pk_mul_f32 v[48:49], v[88:89], v[90:91]\n\t"
      "v_pk_add_f32 v[52:53], v[88:89], v[90:91]\n\t"
      "v_rcp_f32_e32 v48, v48\n\t"
      "v_rcp_f32_e32 v49, v49\n\t"
      "v_pk_fma_f32 v[50:51], v[104:105], v[90:91], v[50:51]\n\t"
      "v_pk_fma_f32 v[42:43], v[52:53], v[48:49], v[42:43]\n\t"
      "v_pk_fma_f32 v[44:45], v[50:51], v[48:49], v[44:45]\n\t"
      "s_cmp_eq_u32 s58, 3\n\t"
      "s_cbranch_scc1 SD010_%=\n\t"
      "v_exp_f32_e64 v92, v92 clamp\n\t"
      "v_exp_f32_e64 v93, v93 clamp\n\t"
      "v_exp_f32_e64 v94, v94 clamp\n\t"
      "v_exp_f32_e64 v95, v95 clamp\n\t"
      "v_pk_fma_f32 v[92:93], v[92:93], s[68:69], v[40:41] op_sel_hi:[1,0,0]\n\t"
      "v_pk_fma_f32 v[94:95], v[94:95], s[68:69], v[40:41] op_sel_hi:[1,0,0]\n\t"
      "v_pk_mul_f32 v[50:51], v[110:111], v[92:93]\n\t"
      "v_pk_mul_f32 v[48:49], v[92:93], v[94:95]\n\t"
      "v_pk_add_f32 v[52:53], v[92:93], v[94:95]\n\t"
      "v_rcp_f32_e32 v48, v48\n\t"
      "v_rcp_f32_e32 v49, v49\n\t"
      "v_pk_fma_f32 v[50:51], v[108:109], v[94:95], v[50:51]\n\t"
      "v_pk_fma_f32 v[42:43], v[52:53], v[48:49], v[42:43]\n\t"
      "v_pk_fma_f32 v[44:45], v[50:51], v[48:49], v[44:45]\n\t"
      "SD010_%=:\n\t"
      "s_waitcnt lgkmcnt(0)\n\t"
      "s_bfe_u32 s75, s56, 0x50018\n\t"
      "v_rcp_f32_e32 v48, v42\n\t"
      "v_rcp_f32_e32 v49, v43\n\t"
      "v_mov_b64_e32 v[42:43], s[72:73]\n\t"
      "v_pk_fma_f32 v[50:51], v[44:45], v[48:49], v[38:39]\n\t"
      "v_mov_b64_e32 v[44:45], 0\n\t"
      "v_max_f32_e32 v50, 0, v50\n\t"
      "v_max_f32_e32 v51, 0, v51\n\t"
      "v_lshl_add_u32 v26, s75, 2, %[gcb]\n\t"
      "v_pk_add_f32 v[50:51], v[50:51], v[36:37]\n\t"
      "s_cmp_gt_u32 s57, s74\n\t"
      "v_cvt_pk_bf16_f32 v52, v50, v51\n\t"
      "ds_write_b16 v34, v52 offset:%c[ao]\n\t"
      "ds_write_b16_d16_hi v35, v52 offset:%c[ao]\n\t"
      "ds_add_rtn_u32 v27, v26, v24\n\t"
      "s_cbranch_scc1 PR010_%=\n\t"
      "PB010_%=:\n\t"
      "v_xor_b32_sdwa v34, v29, v32 dst_sel:DWORD dst_unused:UNUSED_PAD src0_sel:WORD_0 src1_sel:DWORD\n\t"
      "v_xor_b32_sdwa v35, v29, v32 dst_sel:DWORD dst_unused:UNUSED_PAD src0_sel:WORD_1 src1_sel:DWORD\n\t"
      "ds_read_u16_d16_hi v46, v34 offset:%c[ao]\n\t"
      "ds_read_u16_d16_hi v47, v35 offset:%c[ao]\n\t"
      "T010_%=:\n\t"
      "s_add_u32 s48, s48, 4\n\t"
      "s_sub_u32 s49, s49, 4\n\t"
      "s_mov_b32 s56, s50\n\t"
      "s_mov_b32 s64, B000_%=-PC_%=\n\t"
      "s_mov_b32 s65, B001_%=-PC_%=\n\t"
      "s_waitcnt vmcnt(0)\n\t"
      "buffer_load_dword v96, v64, s[40:43], 0 offen sc1\n\t"
      "buffer_load_dword v97, v65, s[40:43], 0 offen sc1\n\t"
      "buffer_load_dword v98, v66, s[40:43], 0 offen sc1\n\t"
      "buffer_load_dword v99, v67, s[40:43], 0 offen sc1\n\t"
      "buffer_load_dword v100, v68, s[40:43], 0 offen sc1\n\t"
      "buffer_load_dword v101, v69, s[40:43], 0 offen sc1\n\t"
      "buffer_load_dword v102, v70, s[40:43], 0 offen sc1\n\t"
      "buffer_load_dword v103, v71, s[40:43], 0 offen sc1\n\t"
      "buffer_load_dword v104, v72, s[40:43], 0 offen sc1\n\t"
      "buffer_load_dword v105, v73, s[40:43], 0 offen sc1\n\t"
      "buffer_load_dword v106, v74, s[40:43], 0 offen sc1\n\t"
      "buffer_load_dword v107, v75, s[40:43], 0 offen sc1\n\t"
      "buffer_load_dword v108, v76, s[40:43], 0 offen sc1\n\t"
      "buffer_load_dword v109, v77, s[40:43], 0 offen sc1\n\t"
      "buffer_load_dword v110, v78, s[40:43], 0 offen sc1\n\t"
      "buffer_load_dword v111, v79, s[40:43], 0 offen sc1\n\t"
      "s_waitcnt lgkmcnt(0)\n\t"
      "v_readfirstlane_b32 s52, v27\n\t"
      "v_pk_add_f32 v[46:47], v[46:47], s[70:71] op_sel_hi:[1,0]\n\t"
      "s_cmp_eq_u32 s52, 12\n\t"
      "s_cbranch_scc1 RP010_%=\n\t"
      "RB010_%=:\n\t"
      "s_cmp_le_u32 s49, 4\n\t"
      "s_cselect_b32 s60, s67, s66\n\t"
      "s_add_u32 s60, s60, s62\n\t"
      "s_addc_u32 s61, s63, 0\n\t"
      "s_setpc_b64 s[60:61]\n\t"
      "RP010_%=:\n\t"
      "v_lshl_add_u32 v25, s75, 4, %[g2b]\n\t"
      "ds_read_b32 v28, v25\n\t"
      "s_waitcnt lgkmcnt(0)\n\t"
      "v_xor_b32_sdwa v26, v28, %[l16] dst_sel:DWORD dst_unused:UNUSED_PAD src0_sel:WORD_0 src1_sel:DWORD\n\t"
      "v_xor_b32_sdwa v25, v28, %[l16] dst_sel:DWORD dst_unused:UNUSED_PAD src0_sel:WORD_1 src1_sel:DWORD\n\t"
      "ds_read_b128 v[48:51], v26 offset:%c[ao]\n\t"
      "s_waitcnt lgkmcnt(0)\n\t"
      "buffer_store_dwordx4 v[48:51], v25, s[44:47], 0 offen sc1\n\t"
      "s_branch RB010_%=\n\t"
      "PR010_%=:\n\t"
      "s_or_b32 s74, s57, 0x1fffffff\n\t"
      "s_lshr_b32 s52, s74, 29\n\t"
      "v_lshl_add_u32 v25, s52, 10, %[wfb]\n\t"
      "v_lshl_or_b32 v32, s52, 6, %[c2]\n\t"
      "ds_read_b128 v[56:59], v25\n\t"
      "s_setprio 2\n\t"
      "s_cmp_eq_u32 s52, 1\n\t"
      "s_cbranch_scc1 PB010_%=\n\t"
      "s_setprio 1\n\t"
      "s_cmp_eq_u32 s52, 2\n\t"
      "s_cbranch_scc1 PB010_%=\n\t"
      "s_setprio 0\n\t"
      "s_branch PB010_%=\n\t"
      ".p2align 6\n\t"
      "B011_%=:\n\t"
      "v_dot2_f32_bf16 v80, v96, s54, v46\n\t"
      "v_dot2_f32_bf16 v81, v97, s54, v47\n\t"
      "v_dot2_f32_bf16 v82, v98, s54, v46\n\t"
      "v_dot2_f32_bf16 v83, v99, s54, v47\n\t"
      "s_cmp_eq_u32 s58, 1\n\t"
      "s_cbranch_scc1 DD011_%=\n\t"
      "v_dot2_f32_bf16 v84, v100, s54, v46\n\t"
      "v_dot2_f32_bf16 v85, v101, s54, v47\n\t"
      "v_dot2_f32_bf16 v86, v102, s54, v46\n\t"
      "v_dot2_f32_bf16 v87, v103, s54, v47\n\t"
      "s_cmp_eq_u32 s58, 2\n\t"
      "s_cbranch_scc1 DD011_%=\n\t"
      "v_dot2_f32_bf16 v88, v104, s54, v46\n\t"
      "v_dot2_f32_bf16 v89, v105, s54, v47\n\t"
      "v_dot2_f32_bf16 v90, v106, s54, v46\n\t"
      "v_dot2_f32_bf16 v91, v107, s54, v47\n\t"
      "s_cmp_eq_u32 s58, 3\n\t"
      "s_cbranch_scc1 DD011_%=\n\t"
      "v_dot2_f32_bf16 v92, v108, s54, v46\n\t"
      "v_dot2_f32_bf16 v93, v109, s54, v47\n\t"
      "v_dot2_f32_bf16 v94, v110, s54, v46\n\t"
      "v_dot2_f32_bf16 v95, v111, s54, v47\n\t"
      "DD011_%=:\n\t"
      "v_add_u32_e32 v26, v31, %[eb]\n\t"
      "s_nop 1\n\t"
      "v_mfma_f32_32x32x16_bf16 v[80:95], v[60:63], v[56:59], v[80:95]\n\t"
      "ds_read_b128 v[60:63], v26\n\t"
      "ds_add_rtn_u32 v30, %[nxa], v24\n\t"
      "s_bfe_u32 s52, s57, 0x50018\n\t"
      "v_lshl_add_u32 v26, s52, 3, %[gnb]\n\t"
      "ds_read_b32 v29, v26\n\t"
      "v_lshl_add_u32 v26, s48, 5, %[qb]\n\t"
      "v_lshl_add_u32 v25, s48, 4, %[eib]\n\t"
      "ds_read_b128 v[64:67], v26 offset:0\n\t"
      "ds_read_b128 v[68:71], v26 offset:32\n\t"
      "ds_read_b128 v[72:75], v26 offset:64\n\t"
      "ds_read_b128 v[76:79], v26 offset:96\n\t"
      "ds_read_u16 v31, v25\n\t"
      "ds_read_u16_d16_hi v38, v34 offset:%c[uo]\n\t"
      "ds_read_u16_d16_hi v39, v35 offset:%c[uo]\n\t"
      "ds_read_u16_d16_hi v36, v34\n\t"
      "ds_read_u16_d16_hi v37, v35\n\t"
      "v_exp_f32_e64 v80, v80 clamp\n\t"
      "v_exp_f32_e64 v81, v81 clamp\n\t"
      "v_exp_f32_e64 v82, v82 clamp\n\t"
      "v_exp_f32_e64 v83, v83 clamp\n\t"
      "v_pk_fma_f32 v[80:81], v[80:81], s[68:69], v[40:41] op_sel_hi:[1,0,0]\n\t"
      "v_pk_fma_f32 v[82:83], v[82:83], s[68:69], v[40:41] op_sel_hi:[1,0,0]\n\t"
      "v_pk_mul_f32 v[50:51], v[98:99], v[80:81]\n\t"
      "v_pk_mul_f32 v[48:49], v[80:81], v[82:83]\n\t"
      "v_pk_add_f32 v[52:53], v[80:81], v[82:83]\n\t"
      "v_rcp_f32_e32 v48, v48\n\t"
      "v_rcp_f32_e32 v49, v49\n\t"
      "v_pk_fma_f32 v[50:51], v[96:97], v[82:83], v[50:51]\n\t"
      "v_pk_fma_f32 v[42:43], v[52:53], v[48:49], v[42:43]\n\t"
      "v_pk_fma_f32 v[44:45], v[50:51], v[48:49], v[44:45]\n\t"
      "s_cmp_eq_u32 s58, 1\n\t"
      "s_cbranch_scc1 SD011_%=\n\t"
      "v_exp_f32_e64 v84, v84 clamp\n\t"
      "v_exp_f32_e64 v85, v85 clamp\n\t"
      "v_exp_f32_e64 v86, v86 clamp\n\t"
      "v_exp_f32_e64 v87, v87 clamp\n\t"
      "v_pk_fma_f32 v[84:85], v[84:85], s[68:69], v[40:41] op_sel_hi:[1,0,0]\n\t"
      "v_pk_fma_f32 v[86:87], v[86:87], s[68:69], v[40:41] op_sel_hi:[1,0,0]\n\t"
      "v_pk_mul_f32 v[50:51], v[102:103], v[84:85]\n\t"
      "v_pk_mul_f32 v[48:49], v[84:85], v[86:87]\n\t"
      "v_pk_add_f32 v[52:53], v[84:85], v[86:87]\n\t"
      "v_rcp_f32_e32 v48, v48\n\t"
      "v_rcp_f32_e32 v49, v49\n\t"
      "v_pk_fma_f32 v[50:51], v[100:101], v[86:87], v[50:51]\n\t"
      "v_pk_fma_f32 v[42:43], v[52:53], v[48:49], v[42:43]\n\t"
      "v_pk_fma_f32 v[44:45], v[50:51], v[48:49], v[44:45]\n\t"
      "s_cmp_eq_u32 s58, 2\n\t"
      "s_cbranch_scc1 SD011_%=\n\t"
      "v_exp_f32_e64 v88, v88 clamp\n\t"
      "v_exp_f32_e64 v89, v89 clamp\n\t"
      "v_exp_f32_e64 v90, v90 clamp\n\t"
      "v_exp_f32_e64 v91, v91 clamp\n\t"
      "v_pk_fma_f32 v[88:89], v[88:89], s[68:69], v[40:41] op_sel_hi:[1,0,0]\n\t"
      "v_pk_fma_f32 v[90:91], v[90:91], s[68:69], v[40:41] op_sel_hi:[1,0,0]\n\t"
      "v_pk_mul_f32 v[50:51], v[106:107], v[88:89]\n\t"
      "v_pk_mul_f32 v[48:49], v[88:89], v[90:91]\n\t"
      "v_pk_add_f32 v[52:53], v[88:89], v[90:91]\n\t"
      "v_rcp_f32_e32 v48, v48\n\t"
      "v_rcp_f32_e32 v49, v49\n\t"
      "v_pk_fma_f32 v[50:51], v[104:105], v[90:91], v[50:51]\n\t"
      "v_pk_fma_f32 v[42:43], v[52:53], v[48:49], v[42:43]\n\t"
      "v_pk_fma_f32 v[44:45], v[50:51], v[48:49], v[44:45]\n\t"
      "s_cmp_eq_u32 s58, 3\n\t"
      "s_cbranch_scc1 SD011_%=\n\t"
      "v_exp_f32_e64 v92, v92 clamp\n\t"
      "v_exp_f32_e64 v93, v93 clamp\n\t"
      "v_exp_f32_e64 v94, v94 clamp\n\t"
      "v_exp_f32_e64 v95, v95 clamp\n\t"
      "v_pk_fma_f32 v[92:93], v[92:93], s[68:69], v[40:41] op_sel_hi:[1,0,0]\n\t"
      "v_pk_fma_f32 v[94:95], v[94:95], s[68:69], v[40:41] op_sel_hi:[1,0,0]\n\t"
      "v_pk_mul_f32 v[50:51], v[110:111], v[92:93]\n\t"
      "v_pk_mul_f32 v[48:49], v[92:93], v[94:95]\n\t"
      "v_pk_add_f32 v[52:53], v[92:93], v[94:95]\n\t"
      "v_rcp_f32_e32 v48, v48\n\t"
      "v_rcp_f32_e32 v49, v49\n\t"
      "v_pk_fma_f32 v[50:51], v[108:109], v[94:95], v[50:51]\n\t"
      "v_pk_fma_f32 v[42:43], v[52:53], v[48:49], v[42:43]\n\t"
      "v_pk_fma_f32 v[44:45], v[50:51], v[48:49], v[44:45]\n\t"
      "SD011_%=:\n\t"
      "s_waitcnt lgkmcnt(0)\n\t"
      "ds_read_b32 v30, v30\n\t"
      "s_bfe_u32 s75, s56, 0x50018\n\t"
      "v_rcp_f32_e32 v48, v42\n\t"
      "v_rcp_f32_e32 v49, v43\n\t"
      "v_mov_b64_e32 v[42:43], s[72:73]\n\t"
      "v_pk_fma_f32 v[50:51], v[44:45], v[48:49], v[38:39]\n\t"
      "v_mov_b64_e32 v[44:45], 0\n\t"
      "v_max_f32_e32 v50, 0, v50\n\t"
      "v_max_f32_e32 v51, 0, v51\n\t"
      "v_lshl_add_u32 v26, s75, 2, %[gcb]\n\t"
      "v_pk_add_f32 v[50:51], v[50:51], v[36:37]\n\t"
      "s_cmp_gt_u32 s57, s74\n\t"
      "v_cvt_pk_bf16_f32 v52, v50, v51\n\t"
      "ds_write_b16 v34, v52 offset:%c[ao]\n\t"
      "ds_write_b16_d16_hi v35, v52 offset:%c[ao]\n\t"
      "ds_add_rtn_u32 v27, v26, v24\n\t"
      "s_cbranch_scc1 PR011_%=\n\t"
      "PB011_%=:\n\t"
      "v_xor_b32_sdwa v34, v29, v32 dst_sel:DWORD dst_unused:UNUSED_PAD src0_sel:WORD_0 src1_sel:DWORD\n\t"
      "v_xor_b32_sdwa v35, v29, v32 dst_sel:DWORD dst_unused:UNUSED_PAD src0_sel:WORD_1 src1_sel:DWORD\n\t"
      "ds_read_u16_d16_hi v46, v34 offset:%c[ao]\n\t"
      "ds_read_u16_d16_hi v47, v35 offset:%c[ao]\n\t"
      "T011_%=:\n\t"
      "s_mov_b32 s56, s50\n\t"
      "s_mov_b32 s58, s49\n\t"
      "s_mov_b32 s64, B010_%=-PC_%=\n\t"
      "s_mov_b32 s65, B011_%=-PC_%=\n\t"
      "s_cmp_eq_u32 s49, 0\n\t"
      "s_cbranch_scc1 X0011_%=\n\t"
      "s_waitcnt vmcnt(0)\n\t"
      "buffer_load_dword v96, v64, s[40:43], 0 offen sc1\n\t"
      "buffer_load_dword v97, v65, s[40:43], 0 offen sc1\n\t"
      "buffer_load_dword v98, v66, s[40:43], 0 offen sc1\n\t"
      "buffer_load_dword v99, v67, s[40:43], 0 offen sc1\n\t"
      "s_cmp_eq_u32 s49, 1\n\t"
      "s_cbranch_scc1 XO011_%=\n\t"
      "buffer_load_dword v100, v68, s[40:43], 0 offen sc1\n\t"
      "buffer_load_dword v101, v69, s[40:43], 0 offen sc1\n\t"
      "buffer_load_dword v102, v70, s[40:43], 0 offen sc1\n\t"
      "buffer_load_dword v103, v71, s[40:43], 0 offen sc1\n\t"
      "s_cmp_eq_u32 s49, 2\n\t"
      "s_cbranch_scc1 XO011_%=\n\t"
      "buffer_load_dword v104, v72, s[40:43], 0 offen sc1\n\t"
      "buffer_load_dword v105, v73, s[40:43], 0 offen sc1\n\t"
      "buffer_load_dword v106, v74, s[40:43], 0 offen sc1\n\t"
      "buffer_load_dword v107, v75, s[40:43], 0 offen sc1\n\t"
      "s_cmp_eq_u32 s49, 3\n\t"
      "s_cbranch_scc1 XO011_%=\n\t"
      "buffer_load_dword v108, v76, s[40:43], 0 offen sc1\n\t"
      "buffer_load_dword v109, v77, s[40:43], 0 offen sc1\n\t"
      "buffer_load_dword v110, v78, s[40:43], 0 offen sc1\n\t"
      "buffer_load_dword v111, v79, s[40:43], 0 offen sc1\n\t"
      "XO011_%=:\n\t"
      "s_waitcnt lgkmcnt(0)\n\t"
      "v_readfirstlane_b32 s50, v30\n\t"
      "s_and_b32 s48, s50, 0x3ff\n\t"
      "s_bfe_u32 s49, s50, 0xa000a\n\t"
      "s_bfe_u32 s52, s50, 0x20014\n\t"
      "s_mul_i32 s52, s52, %c[wstr]\n\t"
      "s_add_u32 s40, s51, s52\n\t"
      "s_addc_u32 s41, s55, 0\n\t"
      "v_readfirstlane_b32 s52, v27\n\t"
      "v_pk_add_f32 v[46:47], v[46:47], s[70:71] op_sel_hi:[1,0]\n\t"
      "s_cmp_eq_u32 s52, 12\n\t"
      "s_cbranch_scc1 RP011_%=\n\t"
      "RB011_%=:\n\t"
      "s_cmp_le_u32 s49, 4\n\t"
      "s_cselect_b32 s60, s67, s66\n\t"
      "s_add_u32 s60, s60, s62\n\t"
      "s_addc_u32 s61, s63, 0\n\t"
      "s_setpc_b64 s[60:61]\n\t"
      "X0011_%=:\n\t"
      "s_mov_b32 s64, END_%=-PC_%=\n\t"
      "s_mov_b32 s65, END_%=-PC_%=\n\t"
      "s_cmp_eq_u32 s64, s66\n\t"
      "s_cbranch_scc1 XO011_%=\n\t"
      "s_waitcnt vmcnt(0)\n\t"
      "s_branch XO011_%=\n\t"
      "RP011_%=:\n\t"
      "v_lshl_add_u32 v25, s75, 4, %[g2b]\n\t"
      "ds_read_b32 v28, v25\n\t"
      "s_waitcnt lgkmcnt(0)\n\t"
      "v_xor_b32_sdwa v26, v28, %[l16] dst_sel:DWORD dst_unused:UNUSED_PAD src0_sel:WORD_0 src1_sel:DWORD\n\t"
      "v_xor_b32_sdwa v25, v28, %[l16] dst_sel:DWORD dst_unused:UNUSED_PAD src0_sel:WORD_1 src1_sel:DWORD\n\t"
      "ds_read_b128 v[48:51], v26 offset:%c[ao]\n\t"
      "s_waitcnt lgkmcnt(0)\n\t"
      "buffer_store_dwordx4 v[48:51], v25, s[44:47], 0 offen sc1\n\t"
      "s_branch RB011_%=\n\t"
      "PR011_%=:\n\t"
      "s_or_b32 s74, s57, 0x1fffffff\n\t"
      "s_lshr_b32 s52, s74, 29\n\t"
      "v_lshl_add_u32 v25, s52, 10, %[wfb]\n\t"
      "v_lshl_or_b32 v32, s52, 6, %[c2]\n\t"
      "ds_read_b128 v[56:59], v25\n\t"
      "s_setprio 2\n\t"
      "s_cmp_eq_u32 s52, 1\n\t"
      "s_cbranch_scc1 PB011_%=\n\t"
      "s_setprio 1\n\t"
      "s_cmp_eq_u32 s52, 2\n\t"
      "s_cbranch_scc1 PB011_%=\n\t"
      "s_setprio 0\n\t"
      "s_branch PB011_%=\n\t"
      ".p2align 6\n\t"
      "B100_%=:\n\t"
      "v_dot2_f32_bf16 v80, v112, s54, v46\n\t"
      "v_dot2_f32_bf16 v81, v113, s54, v47\n\t"
      "v_dot2_f32_bf16 v82, v114, s54, v46\n\t"
      "v_dot2_f32_bf16 v83, v115, s54, v47\n\t"
      "v_dot2_f32_bf16 v84, v116, s54, v46\n\t"
      "v_dot2_f32_bf16 v85, v117, s54, v47\n\t"
      "v_dot2_f32_bf16 v86, v118, s54, v46\n\t"
      "v_dot2_f32_bf16 v87, v119, s54, v47\n\t"
      "v_dot2_f32_bf16 v88, v120, s54, v46\n\t"
      "v_dot2_f32_bf16 v89, v121, s54, v47\n\t"
      "v_dot2_f32_bf16 v90, v122, s54, v46\n\t"
      "v_dot2_f32_bf16 v91, v123, s54, v47\n\t"
      "v_dot2_f32_bf16 v92, v124, s54, v46\n\t"
      "v_dot2_f32_bf16 v93, v125, s54, v47\n\t"
      "v_dot2_f32_bf16 v94, v126, s54, v46\n\t"
      "v_dot2_f32_bf16 v95, v127, s54, v47\n\t"
      "DD100_%=:\n\t"
      "v_add_u32_e32 v26, v31, %[eb]\n\t"
      "s_nop 1\n\t"
      "v_mfma_f32_32x32x16_bf16 v[80:95], v[60:63], v[56:59], v[80:95]\n\t"
      "ds_read_b128 v[60:63], v26\n\t"
      "v_lshl_add_u32 v26, s48, 5, %[qb]\n\t"
      "v_lshl_add_u32 v25, s48, 4, %[eib]\n\t"
      "ds_read_b128 v[64:67], v26 offset:0\n\t"
      "ds_read_b128 v[68:71], v26 offset:32\n\t"
      "ds_read_b128 v[72:75], v26 offset:64\n\t"
      "ds_read_b128 v[76:79], v26 offset:96\n\t"
      "ds_read_u16 v31, v25\n\t"
      "s_add_u32 s48, s48, 4\n\t"
      "s_sub_u32 s49, s49, 4\n\t"
      "s_mov_b32 s57, s50\n\t"
      "s_mov_b32 s66, B100_%=-PC_%=\n\t"
      "s_mov_b32 s67, B101_%=-PC_%=\n\t"
      "v_exp_f32_e64 v80, v80 clamp\n\t"
      "v_exp_f32_e64 v81, v81 clamp\n\t"
      "v_exp_f32_e64 v82, v82 clamp\n\t"
      "v_exp_f32_e64 v83, v83 clamp\n\t"
      "v_exp_f32_e64 v84, v84 clamp\n\t"
      "v_exp_f32_e64 v85, v85 clamp\n\t"
      "v_exp_f32_e64 v86, v86 clamp\n\t"
      "v_exp_f32_e64 v87, v87 clamp\n\t"
      "v_pk_fma_f32 v[80:81], v[80:81], s[68:69], v[40:41] op_sel_hi:[1,0,0]\n\t"
      "v_pk_fma_f32 v[82:83], v[82:83], s[68:69], v[40:41] op_sel_hi:[1,0,0]\n\t"
      "v_pk_fma_f32 v[84:85], v[84:85], s[68:69], v[40:41] op_sel_hi:[1,0,0]\n\t"
      "v_pk_fma_f32 v[86:87], v[86:87], s[68:69], v[40:41] op_sel_hi:[1,0,0]\n\t"
      "v_pk_mul_f32 v[48:49], v[80:81], v[82:83]\n\t"
      "v_pk_mul_f32 v[50:51], v[84:85], v[86:87]\n\t"
      "v_pk_mul_f32 v[114:115], v[114:115], v[80:81]\n\t"
      "v_pk_mul_f32 v[52:53], v[48:49], v[50:51]\n\t"
      "v_pk_mul_f32 v[118:119], v[118:119], v[84:85]\n\t"
      "v_rcp_f32_e32 v52, v52\n\t"
      "v_rcp_f32_e32 v53, v53\n\t"
      "v_pk_fma_f32 v[54:55], v[112:113], v[82:83], v[114:115]\n\t"
      "v_pk_add_f32 v[80:81], v[80:81], v[82:83]\n\t"
      "v_pk_fma_f32 v[82:83], v[116:117], v[86:87], v[118:119]\n\t"
      "v_pk_mul_f32 v[50:51], v[50:51], v[52:53]\n\t"
      "v_pk_mul_f32 v[48:49], v[48:49], v[52:53]\n\t"
      "v_pk_add_f32 v[84:85], v[84:85], v[86:87]\n\t"
      "v_pk_fma_f32 v[44:45], v[54:55], v[50:51], v[44:45]\n\t"
      "v_pk_fma_f32 v[42:43], v[80:81], v[50:51], v[42:43]\n\t"
      "v_pk_fma_f32 v[44:45], v[82:83], v[48:49], v[44:45]\n\t"
      "v_pk_fma_f32 v[42:43], v[84:85], v[48:49], v[42:43]\n\t"
      "v_exp_f32_e64 v88, v88 clamp\n\t"
      "v_exp_f32_e64 v89, v89 clamp\n\t"
      "v_exp_f32_e64 v90, v90 clamp\n\t"
      "v_exp_f32_e64 v91, v91 clamp\n\t"
      "v_exp_f32_e64 v92, v92 clamp\n\t"
      "v_exp_f32_e64 v93, v93 clamp\n\t"
      "v_exp_f32_e64 v94, v94 clamp\n\t"
      "v_exp_f32_e64 v95, v95 clamp\n\t"
      "v_pk_fma_f32 v[88:89], v[88:89], s[68:69], v[40:41] op_sel_hi:[1,0,0]\n\t"
      "v_pk_fma_f32 v[90:91], v[90:91], s[68:69], v[40:41] op_sel_hi:[1,0,0]\n\t"
      "v_pk_fma_f32 v[92:93], v[92:93], s[68:69], v[40:41] op_sel_hi:[1,0,0]\n\t"
      "v_pk_fma_f32 v[94:95], v[94:95], s[68:69], v[40:41] op_sel_hi:[1,0,0]\n\t"
      "v_pk_mul_f32 v[48:49], v[88:89], v[90:91]\n\t"
      "v_pk_mul_f32 v[50:51], v[92:93], v[94:95]\n\t"
      "v_pk_mul_f32 v[122:123], v[122:123], v[88:89]\n\t"
      "v_pk_mul_f32 v[52:53], v[48:49], v[50:51]\n\t"
      "v_pk_mul_f32 v[126:127], v[126:127], v[92:93]\n\t"
      "v_rcp_f32_e32 v52, v52\n\t"
      "v_rcp_f32_e32 v53, v53\n\t"
      "v_pk_fma_f32 v[54:55], v[120:121], v[90:91], v[122:123]\n\t"
      "v_pk_add_f32 v[88:89], v[88:89], v[90:91]\n\t"
      "v_pk_fma_f32 v[90:91], v[124:125], v[94:95], v[126:127]\n\t"
      "v_pk_mul_f32 v[50:51], v[50:51], v[52:53]\n\t"
      "v_pk_mul_f32 v[48:49], v[48:49], v[52:53]\n\t"
      "v_pk_add_f32 v[92:93], v[92:93], v[94:95]\n\t"
      "v_pk_fma_f32 v[44:45], v[54:55], v[50:51], v[44:45]\n\t"
      "v_pk_fma_f32 v[42:43], v[88:89], v[50:51], v[42:43]\n\t"
      "v_pk_fma_f32 v[44:45], v[90:91], v[48:49], v[44:45]\n\t"
      "v_pk_fma_f32 v[42:43], v[92:93], v[48:49], v[42:43]\n\t"
      "T100_%=:\n\t"
      "s_waitcnt vmcnt(0) lgkmcnt(0)\n\t"
      "buffer_load_dword v112, v64, s[40:43], 0 offen sc1\n\t"
      "buffer_load_dword v113, v65, s[40:43], 0 offen sc1\n\t"
      "buffer_load_dword v114, v66, s[40:43], 0 offen sc1\n\t"
      "buffer_load_dword v115, v67, s[40:43], 0 offen sc1\n\t"
      "buffer_load_dword v116, v68, s[40:43], 0 offen sc1\n\t"
      "buffer_load_dword v117, v69, s[40:43], 0 offen sc1\n\t"
      "buffer_load_dword v118, v70, s[40:43], 0 offen sc1\n\t"
      "buffer_load_dword v119, v71, s[40:43], 0 offen sc1\n\t"
      "buffer_load_dword v120, v72, s[40:43], 0 offen sc1\n\t"
      "buffer_load_dword v121, v73, s[40:43], 0 offen sc1\n\t"
      "buffer_load_dword v122, v74, s[40:43], 0 offen sc1\n\t"
      "buffer_load_dword v123, v75, s[40:43], 0 offen sc1\n\t"
      "buffer_load_dword v124, v76, s[40:43], 0 offen sc1\n\t"
      "buffer_load_dword v125, v77, s[40:43], 0 offen sc1\n\t"
      "buffer_load_dword v126, v78, s[40:43], 0 offen sc1\n\t"
      "buffer_load_dword v127, v79, s[40:43], 0 offen sc1\n\t"
      "s_cmp_le_u32 s49, 4\n\t"
      "s_cselect_b32 s60, s65, s64\n\t"
      "s_add_u32 s60, s60, s62\n\t"
      "s_addc_u32 s61, s63, 0\n\t"
      "s_setpc_b64 s[60:61]\n\t"
      ".p2align 6\n\t"
      "B101_%=:\n\t"
      "v_dot2_f32_bf16 v80, v112, s54, v46\n\t"
      "v_dot2_f32_bf16 v81, v113, s54, v47\n\t"
      "v_dot2_f32_bf16 v82, v114, s54, v46\n\t"
      "v_dot2_f32_bf16 v83, v115, s54, v47\n\t"
      "v_dot2_f32_bf16 v84, v116, s54, v46\n\t"
      "v_dot2_f32_bf16 v85, v117, s54, v47\n\t"
      "v_dot2_f32_bf16 v86, v118, s54, v46\n\t"
      "v_dot2_f32_bf16 v87, v119, s54, v47\n\t"
      "v_dot2_f32_bf16 v88, v120, s54, v46\n\t"
      "v_dot2_f32_bf16 v89, v121, s54, v47\n\t"
      "v_dot2_f32_bf16 v90, v122, s54, v46\n\t"
      "v_dot2_f32_bf16 v91, v123, s54, v47\n\t"
      "v_dot2_f32_bf16 v92, v124, s54, v46\n\t"
      "v_dot2_f32_bf16 v93, v125, s54, v47\n\t"
      "v_dot2_f32_bf16 v94, v126, s54, v46\n\t"
      "v_dot2_f32_bf16 v95, v127, s54, v47\n\t"
      "DD101_%=:\n\t"
      "v_add_u32_e32 v26, v31, %[eb]\n\t"
      "s_nop 1\n\t"
      "v_mfma_f32_32x32x16_bf16 v[80:95], v[60:63], v[56:59], v[80:95]\n\t"
      "ds_read_b128 v[60:63], v26\n\t"
      "ds_add_rtn_u32 v30, %[nxa], v24\n\t"
      "v_lshl_add_u32 v26, s48, 5, %[qb]\n\t"
      "v_lshl_add_u32 v25, s48, 4, %[eib]\n\t"
      "ds_read_b128 v[64:67], v26 offset:0\n\t"
      "ds_read_b128 v[68:71], v26 offset:32\n\t"
      "ds_read_b128 v[72:75], v26 offset:64\n\t"
      "ds_read_b128 v[76:79], v26 offset:96\n\t"
      "ds_read_u16 v31, v25\n\t"
      "s_mov_b32 s57, s50\n\t"
      "s_mov_b32 s59, s49\n\t"
      "s_mov_b32 s66, B110_%=-PC_%=\n\t"
      "s_mov_b32 s67, B111_%=-PC_%=\n\t"
      "v_exp_f32_e64 v80, v80 clamp\n\t"
      "v_exp_f32_e64 v81, v81 clamp\n\t"
      "v_exp_f32_e64 v82, v82 clamp\n\t"
      "v_exp_f32_e64 v83, v83 clamp\n\t"
      "v_exp_f32_e64 v84, v84 clamp\n\t"
      "v_exp_f32_e64 v85, v85 clamp\n\t"
      "v_exp_f32_e64 v86, v86 clamp\n\t"
      "v_exp_f32_e64 v87, v87 clamp\n\t"
      "v_pk_fma_f32 v[80:81], v[80:81], s[68:69], v[40:41] op_sel_hi:[1,0,0]\n\t"
      "v_pk_fma_f32 v[82:83], v[82:83], s[68:69], v[40:41] op_sel_hi:[1,0,0]\n\t"
      "v_pk_fma_f32 v[84:85], v[84:85], s[68:69], v[40:41] op_sel_hi:[1,0,0]\n\t"
      "v_pk_fma_f32 v[86:87], v[86:87], s[68:69], v[40:41] op_sel_hi:[1,0,0]\n\t"
      "v_pk_mul_f32 v[48:49], v[80:81], v[82:83]\n\t"
      "v_pk_mul_f32 v[50:51], v[84:85], v[86:87]\n\t"
      "v_pk_mul_f32 v[114:115], v[114:115], v[80:81]\n\t"
      "v_pk_mul_f32 v[52:53], v[48:49], v[50:51]\n\t"
      "v_pk_mul_f32 v[118:119], v[118:119], v[84:85]\n\t"
      "v_rcp_f32_e32 v52, v52\n\t"
      "v_rcp_f32_e32 v53, v53\n\t"
      "v_pk_fma_f32 v[54:55], v[112:113], v[82:83], v[114:115]\n\t"
      "v_pk_add_f32 v[80:81], v[80:81], v[82:83]\n\t"
      "v_pk_fma_f32 v[82:83], v[116:117], v[86:87], v[118:119]\n\t"
      "v_pk_mul_f32 v[50:51], v[50:51], v[52:53]\n\t"
      "v_pk_mul_f32 v[48:49], v[48:49], v[52:53]\n\t"
      "v_pk_add_f32 v[84:85], v[84:85], v[86:87]\n\t"
      "v_pk_fma_f32 v[44:45], v[54:55], v[50:51], v[44:45]\n\t"
      "v_pk_fma_f32 v[42:43], v[80:81], v[50:51], v[42:43]\n\t"
      "v_pk_fma_f32 v[44:45], v[82:83], v[48:49], v[44:45]\n\t"
      "v_pk_fma_f32 v[42:43], v[84:85], v[48:49], v[42:43]\n\t"
      "s_waitcnt lgkmcnt(0)\n\t"
      "ds_read_b32 v30, v30\n\t"
      "v_exp_f32_e64 v88, v88 clamp\n\t"
      "v_exp_f32_e64 v89, v89 clamp\n\t"
      "v_exp_f32_e64 v90, v90 clamp\n\t"
      "v_exp_f32_e64 v91, v91 clamp\n\t"
      "v_exp_f32_e64 v92, v92 clamp\n\t"
      "v_exp_f32_e64 v93, v93 clamp\n\t"
      "v_exp_f32_e64 v94, v94 clamp\n\t"
      "v_exp_f32_e64 v95, v95 clamp\n\t"
      "v_pk_fma_f32 v[88:89], v[88:89], s[68:69], v[40:41] op_sel_hi:[1,0,0]\n\t"
      "v_pk_fma_f32 v[90:91], v[90:91], s[68:69], v[40:41] op_sel_hi:[1,0,0]\n\t"
      "v_pk_fma_f32 v[92:93], v[92:93], s[68:69], v[40:41] op_sel_hi:[1,0,0]\n\t"
      "v_pk_fma_f32 v[94:95], v[94:95], s[68:69], v[40:41] op_sel_hi:[1,0,0]\n\t"
      "v_pk_mul_f32 v[48:49], v[88:89], v[90:91]\n\t"
      "v_pk_mul_f32 v[50:51], v[92:93], v[94:95]\n\t"
      "v_pk_mul_f32 v[122:123], v[122:123], v[88:89]\n\t"
      "v_pk_mul_f32 v[52:53], v[48:49], v[50:51]\n\t"
      "v_pk_mul_f32 v[126:127], v[126:127], v[92:93]\n\t"
      "v_rcp_f32_e32 v52, v52\n\t"
      "v_rcp_f32_e32 v53, v53\n\t"
      "v_pk_fma_f32 v[54:55], v[120:121], v[90:91], v[122:123]\n\t"
      "v_pk_add_f32 v[88:89], v[88:89], v[90:91]\n\t"
      "v_pk_fma_f32 v[90:91], v[124:125], v[94:95], v[126:127]\n\t"
      "v_pk_mul_f32 v[50:51], v[50:51], v[52:53]\n\t"
      "v_pk_mul_f32 v[48:49], v[48:49], v[52:53]\n\t"
      "v_pk_add_f32 v[92:93], v[92:93], v[94:95]\n\t"
      "v_pk_fma_f32 v[44:45], v[54:55], v[50:51], v[44:45]\n\t"
      "v_pk_fma_f32 v[42:43], v[88:89], v[50:51], v[42:43]\n\t"
      "v_pk_fma_f32 v[44:45], v[90:91], v[48:49], v[44:45]\n\t"
      "v_pk_fma_f32 v[42:43], v[92:93], v[48:49], v[42:43]\n\t"
      "T101_%=:\n\t"
      "s_cmp_eq_u32 s49, 0\n\t"
      "s_cbranch_scc1 X0101_%=\n\t"
      "s_waitcnt vmcnt(0)\n\t"
      "buffer_load_dword v112, v64, s[40:43], 0 offen sc1\n\t"
      "buffer_load_dword v113, v65, s[40:43], 0 offen sc1\n\t"
      "buffer_load_dword v114, v66, s[40:43], 0 offen sc1\n\t"
      "buffer_load_dword v115, v67, s[40:43], 0 offen sc1\n\t"
      "s_cmp_eq_u32 s49, 1\n\t"
      "s_cbranch_scc1 XO101_%=\n\t"
      "buffer_load_dword v116, v68, s[40:43], 0 offen sc1\n\t"
      "buffer_load_dword v117, v69, s[40:43], 0 offen sc1\n\t"
      "buffer_load_dword v118, v70, s[40:43], 0 offen sc1\n\t"
      "buffer_load_dword v119, v71, s[40:43], 0 offen sc1\n\t"
      "s_cmp_eq_u32 s49, 2\n\t"
      "s_cbranch_scc1 XO101_%=\n\t"
      "buffer_load_dword v120, v72, s[40:43], 0 offen sc1\n\t"
      "buffer_load_dword v121, v73, s[40:43], 0 offen sc1\n\t"
      "buffer_load_dword v122, v74, s[40:43], 0 offen sc1\n\t"
      "buffer_load_dword v123, v75, s[40:43], 0 offen sc1\n\t"
      "s_cmp_eq_u32 s49, 3\n\t"
      "s_cbranch_scc1 XO101_%=\n\t"
      "buffer_load_dword v124, v76, s[40:43], 0 offen sc1\n\t"
      "buffer_load_dword v125, v77, s[40:43], 0 offen sc1\n\t"
      "buffer_load_dword v126, v78, s[40:43], 0 offen sc1\n\t"
      "buffer_load_dword v127, v79, s[40:43], 0 offen sc1\n\t"
      "XO101_%=:\n\t"
      "s_waitcnt lgkmcnt(0)\n\t"
      "v_readfirstlane_b32 s50, v30\n\t"
      "s_and_b32 s48, s50, 0x3ff\n\t"
      "s_bfe_u32 s49, s50, 0xa000a\n\t"
      "s_bfe_u32 s52, s50, 0x20014\n\t"
      "s_mul_i32 s52, s52, %c[wstr]\n\t"
      "s_add_u32 s40, s51, s52\n\t"
      "s_addc_u32 s41, s55, 0\n\t"
      "s_cmp_le_u32 s49, 4\n\t"
      "s_cselect_b32 s60, s65, s64\n\t"
      "s_add_u32 s60, s60, s62\n\t"
      "s_addc_u32 s61, s63, 0\n\t"
      "s_setpc_b64 s[60:61]\n\t"
      "X0101_%=:\n\t"
      "s_mov_b32 s66, END_%=-PC_%=\n\t"
      "s_mov_b32 s67, END_%=-PC_%=\n\t"
      "s_cmp_eq_u32 s66, s64\n\t"
      "s_cbranch_scc1 XO101_%=\n\t"
      "s_waitcnt vmcnt(0)\n\t"
      "s_branch XO101_%=\n\t"
      ".p2align 6\n\t"
      "B110_%=:\n\t"
      "v_dot2_f32_bf16 v80, v112, s54, v46\n\t"
      "v_dot2_f32_bf16 v81, v113, s54, v47\n\t"
      "v_dot2_f32_bf16 v82, v114, s54, v46\n\t"
      "v_dot2_f32_bf16 v83, v115, s54, v47\n\t"
      "s_cmp_eq_u32 s59, 1\n\t"
      "s_cbranch_scc1 DD110_%=\n\t"
      "v_dot2_f32_bf16 v84, v116, s54, v46\n\t"
      "v_dot2_f32_bf16 v85, v117, s54, v47\n\t"
      "v_dot2_f32_bf16 v86, v118, s54, v46\n\t"
      "v_dot2_f32_bf16 v87, v119, s54, v47\n\t"
      "s_cmp_eq_u32 s59, 2\n\t"
      "s_cbranch_scc1 DD110_%=\n\t"
      "v_dot2_f32_bf16 v88, v120, s54, v46\n\t"
      "v_dot2_f32_bf16 v89, v121, s54, v47\n\t"
      "v_dot2_f32_bf16 v90, v122, s54, v46\n\t"
      "v_dot2_f32_bf16 v91, v123, s54, v47\n\t"
      "s_cmp_eq_u32 s59, 3\n\t"
      "s_cbranch_scc1 DD110_%=\n\t"
      "v_dot2_f32_bf16 v92, v124, s54, v46\n\t"
      "v_dot2_f32_bf16 v93, v125, s54, v47\n\t"
      "v_dot2_f32_bf16 v94, v126, s54, v46\n\t"
      "v_dot2_f32_bf16 v95, v127, s54, v47\n\t"
      "DD110_%=:\n\t"
      "v_add_u32_e32 v26, v31, %[eb]\n\t"
      "s_nop 1\n\t"
      "v_mfma_f32_32x32x16_bf16 v[80:95], v[60:63], v[56:59], v[80:95]\n\t"
      "ds_read_b128 v[60:63], v26\n\t"
      "s_bfe_u32 s52, s56, 0x50018\n\t"
      "v_lshl_add_u32 v26, s52, 3, %[gnb]\n\t"
      "ds_read_b32 v29, v26\n\t"
      "v_lshl_add_u32 v26, s48, 5, %[qb]\n\t"
      "v_lshl_add_u32 v25, s48, 4, %[eib]\n\t"
      "ds_read_b128 v[64:67], v26 offset:0\n\t"
      "ds_read_b128 v[68:71], v26 offset:32\n\t"
      "ds_read_b128 v[72:75], v26 offset:64\n\t"
      "ds_read_b128 v[76:79], v26 offset:96\n\t"
      "ds_read_u16 v31, v25\n\t"
      "ds_read_u16_d16_hi v38, v34 offset:%c[uo]\n\t"
      "ds_read_u16_d16_hi v39, v35 offset:%c[uo]\n\t"
      "ds_read_u16_d16_hi v36, v34\n\t"
      "ds_read_u16_d16_hi v37, v35\n\t"
      "v_exp_f32_e64 v80, v80 clamp\n\t"
      "v_exp_f32_e64 v81, v81 clamp\n\t"
      "v_exp_f32_e64 v82, v82 clamp\n\t"
      "v_exp_f32_e64 v83, v83 clamp\n\t"
      "v_pk_fma_f32 v[80:81], v[80:81], s[68:69], v[40:41] op_sel_hi:[1,0,0]\n\t"
      "v_pk_fma_f32 v[82:83], v[82:83], s[68:69], v[40:41] op_sel_hi:[1,0,0]\n\t"
      "v_pk_mul_f32 v[50:51], v[114:115], v[80:81]\n\t"
      "v_pk_mul_f32 v[48:49], v[80:81], v[82:83]\n\t"
      "v_pk_add_f32 v[52:53], v[80:81], v[82:83]\n\t"
      "v_rcp_f32_e32 v48, v48\n\t"
      "v_rcp_f32_e32 v49, v49\n\t"
      "v_pk_fma_f32 v[50:51], v[112:113], v[82:83], v[50:51]\n\t"
      "v_pk_fma_f32 v[42:43], v[52:53], v[48:49], v[42:43]\n\t"
      "v_pk_fma_f32 v[44:45], v[50:51], v[48:49], v[44:45]\n\t"
      "s_cmp_eq_u32 s59, 1\n\t"
      "s_cbranch_scc1 SD110_%=\n\t"
      "v_exp_f32_e64 v84, v84 clamp\n\t"
      "v_exp_f32_e64 v85, v85 clamp\n\t"
      "v_exp_f32_e64 v86, v86 clamp\n\t"
      "v_exp_f32_e64 v87, v87 clamp\n\t"
      "v_pk_fma_f32 v[84:85], v[84:85], s[68:69], v[40:41] op_sel_hi:[1,0,0]\n\t"
      "v_pk_fma_f32 v[86:87], v[86:87], s[68:69], v[40:41] op_sel_hi:[1,0,0]\n\t"
      "v_pk_mul_f32 v[50:51], v[118:119], v[84:85]\n\t"
      "v_pk_mul_f32 v[48:49], v[84:85], v[86:87]\n\t"
      "v_pk_add_f32 v[52:53], v[84:85], v[86:87]\n\t"
      "v_rcp_f32_e32 v48, v48\n\t"
      "v_rcp_f32_e32 v49, v49\n\t"
      "v_pk_fma_f32 v[50:51], v[116:117], v[86:87], v[50:51]\n\t"
      "v_pk_fma_f32 v[42:43], v[52:53], v[48:49], v[42:43]\n\t"
      "v_pk_fma_f32 v[44:45], v[50:51], v[48:49], v[44:45]\n\t"
      "s_cmp_eq_u32 s59, 2\n\t"
      "s_cbranch_scc1 SD110_%=\n\t"
      "v_exp_f32_e64 v88, v88 clamp\n\t"
      "v_exp_f32_e64 v89, v89 clamp\n\t"
      "v_exp_f32_e64 v90, v90 clamp\n\t"
      "v_exp_f32_e64 v91, v91 clamp\n\t"
      "v_pk_fma_f32 v[88:89], v[88:89], s[68:69], v[40:41] op_sel_hi:[1,0,0]\n\t"
      "v_pk_fma_f32 v[90:91], v[90:91], s[68:69], v[40:41] op_sel_hi:[1,0,0]\n\t"
      "v_pk_mul_f32 v[50:51], v[122:123], v[88:89]\n\t"
      "v_pk_mul_f32 v[48:49], v[88:89], v[90:91]\n\t"
      "v_pk_add_f32 v[52:53], v[88:89], v[90:91]\n\t"
      "v_rcp_f32_e32 v48, v48\n\t"
      "v_rcp_f32_e32 v49, v49\n\t"
      "v_pk_fma_f32 v[50:51], v[120:121], v[90:91], v[50:51]\n\t"
      "v_pk_fma_f32 v[42:43], v[52:53], v[48:49], v[42:43]\n\t"
      "v_pk_fma_f32 v[44:45], v[50:51], v[48:49], v[44:45]\n\t"
      "s_cmp_eq_u32 s59, 3\n\t"
      "s_cbranch_scc1 SD110_%=\n\t"
      "v_exp_f32_e64 v92, v92 clamp\n\t"
      "v_exp_f32_e64 v93, v93 clamp\n\t"
      "v_exp_f32_e64 v94, v94 clamp\n\t"
      "v_exp_f32_e64 v95, v95 clamp\n\t"
      "v_pk_fma_f32 v[92:93], v[92:93], s[68:69], v[40:41] op_sel_hi:[1,0,0]\n\t"
      "v_pk_fma_f32 v[94:95], v[94:95], s[68:69], v[40:41] op_sel_hi:[1,0,0]\n\t"
      "v_pk_mul_f32 v[50:51], v[126:127], v[92:93]\n\t"
      "v_pk_mul_f32 v[48:49], v[92:93], v[94:95]\n\t"
      "v_pk_add_f32 v[52:53], v[92:93], v[94:95]\n\t"
      "v_rcp_f32_e32 v48, v48\n\t"
      "v_rcp_f32_e32 v49, v49\n\t"
      "v_pk_fma_f32 v[50:51], v[124:125], v[94:95], v[50:51]\n\t"
      "v_pk_fma_f32 v[42:43], v[52:53], v[48:49], v[42:43]\n\t"
      "v_pk_fma_f32 v[44:45], v[50:51], v[48:49], v[44:45]\n\t"
      "SD110_%=:\n\t"
      "s_waitcnt lgkmcnt(0)\n\t"
      "s_bfe_u32 s75, s57, 0x50018\n\t"
      "v_rcp_f32_e32 v48, v42\n\t"
      "v_rcp_f32_e32 v49, v43\n\t"
      "v_mov_b64_e32 v[42:43], s[72:73]\n\t"
      "v_pk_fma_f32 v[50:51], v[44:45], v[48:49], v[38:39]\n\t"
      "v_mov_b64_e32 v[44:45], 0\n\t"
      "v_max_f32_e32 v50, 0, v50\n\t"
      "v_max_f32_e32 v51, 0, v51\n\t"
      "v_lshl_add_u32 v26, s75, 2, %[gcb]\n\t"
      "v_pk_add_f32 v[50:51], v[50:51], v[36:37]\n\t"
      "s_cmp_gt_u32 s56, s74\n\t"
      "v_cvt_pk_bf16_f32 v52, v50, v51\n\t"
      "ds_write_b16 v34, v52 offset:%c[ao]\n\t"
      "ds_write_b16_d16_hi v35, v52 offset:%c[ao]\n\t"
      "ds_add_rtn_u32 v27, v26, v24\n\t"
      "s_cbranch_scc1 PR110_%=\n\t"
      "PB110_%=:\n\t"
      "v_xor_b32_sdwa v34, v29, v32 dst_sel:DWORD dst_unused:UNUSED_PAD src0_sel:WORD_0 src1_sel:DWORD\n\t"
      "v_xor_b32_sdwa v35, v29, v32 dst_sel:DWORD dst_unused:UNUSED_PAD src0_sel:WORD_1 src1_sel:DWORD\n\t"
      "ds_read_u16_d16_hi v46, v34 offset:%c[ao]\n\t"
      "ds_read_u16_d16_hi v47, v35 offset:%c[ao]\n\t"
      "T110_%=:\n\t"
      "s_add_u32 s48, s48, 4\n\t"
      "s_sub_u32 s49, s49, 4\n\t"
      "s_mov_b32 s57, s50\n\t"
      "s_mov_b32 s66, B100_%=-PC_%=\n\t"
      "s_mov_b32 s67, B101_%=-PC_%=\n\t"
      "s_waitcnt vmcnt(0)\n\t"
      "buffer_load_dword v112, v64, s[40:43], 0 offen sc1\n\t"
      "buffer_load_dword v113, v65, s[40:43], 0 offen sc1\n\t"
      "buffer_load_dword v114, v66, s[40:43], 0 offen sc1\n\t"
      "buffer_load_dword v115, v67, s[40:43], 0 offen sc1\n\t"
      "buffer_load_dword v116, v68, s[40:43], 0 offen sc1\n\t"
      "buffer_load_dword v117, v69, s[40:43], 0 offen sc1\n\t"
      "buffer_load_dword v118, v70, s[40:43], 0 offen sc1\n\t"
      "buffer_load_dword v119, v71, s[40:43], 0 offen sc1\n\t"
      "buffer_load_dword v120, v72, s[40:43], 0 offen sc1\n\t"
      "buffer_load_dword v121, v73, s[40:43], 0 offen sc1\n\t"
      "buffer_load_dword v122, v74, s[40:43], 0 offen sc1\n\t"
      "buffer_load_dword v123, v75, s[40:43], 0 offen sc1\n\t"
      "buffer_load_dword v124, v76, s[40:43], 0 offen sc1\n\t"
      "buffer_load_dword v125, v77, s[40:43], 0 offen sc1\n\t"
      "buffer_load_dword v126, v78, s[40:43], 0 offen sc1\n\t"
      "buffer_load_dword v127, v79, s[40:43], 0 offen sc1\n\t"
      "s_waitcnt lgkmcnt(0)\n\t"
      "v_readfirstlane_b32 s52, v27\n\t"
      "v_pk_add_f32 v[46:47], v[46:47], s[70:71] op_sel_hi:[1,0]\n\t"
      "s_cmp_eq_u32 s52, 12\n\t"
      "s_cbranch_scc1 RP110_%=\n\t"
      "RB110_%=:\n\t"
      "s_cmp_le_u32 s49, 4\n\t"
      "s_cselect_b32 s60, s65, s64\n\t"
      "s_add_u32 s60, s60, s62\n\t"
      "s_addc_u32 s61, s63, 0\n\t"
      "s_setpc_b64 s[60:61]\n\t"
      "RP110_%=:\n\t"
      "v_lshl_add_u32 v25, s75, 4, %[g2b]\n\t"
      "ds_read_b32 v28, v25\n\t"
      "s_waitcnt lgkmcnt(0)\n\t"
      "v_xor_b32_sdwa v26, v28, %[l16] dst_sel:DWORD dst_unused:UNUSED_PAD src0_sel:WORD_0 src1_sel:DWORD\n\t"
      "v_xor_b32_sdwa v25, v28, %[l16] dst_sel:DWORD dst_unused:UNUSED_PAD src0_sel:WORD_1 src1_sel:DWORD\n\t"
      "ds_read_b128 v[48:51], v26 offset:%c[ao]\n\t"
      "s_waitcnt lgkmcnt(0)\n\t"
      "buffer_store_dwordx4 v[48:51], v25, s[44:47], 0 offen sc1\n\t"
      "s_branch RB110_%=\n\t"
      "PR110_%=:\n\t"
      "s_or_b32 s74, s56, 0x1fffffff\n\t"
      "s_lshr_b32 s52, s74, 29\n\t"
      "v_lshl_add_u32 v25, s52, 10, %[wfb]\n\t"
      "v_lshl_or_b32 v32, s52, 6, %[c2]\n\t"
      "ds_read_b128 v[56:59], v25\n\t"
      "s_setprio 2\n\t"
      "s_cmp_eq_u32 s52, 1\n\t"
      "s_cbranch_scc1 PB110_%=\n\t"
      "s_setprio 1\n\t"
      "s_cmp_eq_u32 s52, 2\n\t"
      "s_cbranch_scc1 PB110_%=\n\t"
      "s_setprio 0\n\t"
      "s_branch PB110_%=\n\t"
      ".p2align 6\n\t"
      "B111_%=:\n\t"
      "v_dot2_f32_bf16 v80, v112, s54, v46\n\t"
      "v_dot2_f32_bf16 v81, v113, s54, v47\n\t"
      "v_dot2_f32_bf16 v82, v114, s54, v46\n\t"
      "v_dot2_f32_bf16 v83, v115, s54, v47\n\t"
      "s_cmp_eq_u32 s59, 1\n\t"
      "s_cbranch_scc1 DD111_%=\n\t"
      "v_dot2_f32_bf16 v84, v116, s54, v46\n\t"
      "v_dot2_f32_bf16 v85, v117, s54, v47\n\t"
      "v_dot2_f32_bf16 v86, v118, s54, v46\n\t"
      "v_dot2_f32_bf16 v87, v119, s54, v47\n\t"
      "s_cmp_eq_u32 s59, 2\n\t"
      "s_cbranch_scc1 DD111_%=\n\t"
      "v_dot2_f32_bf16 v88, v120, s54, v46\n\t"
      "v_dot2_f32_bf16 v89, v121, s54, v47\n\t"
      "v_dot2_f32_bf16 v90, v122, s54, v46\n\t"
      "v_dot2_f32_bf16 v91, v123, s54, v47\n\t"
      "s_cmp_eq_u32 s59, 3\n\t"
      "s_cbranch_scc1 DD111_%=\n\t"
      "v_dot2_f32_bf16 v92, v124, s54, v46\n\t"
      "v_dot2_f32_bf16 v93, v125, s54, v47\n\t"
      "v_dot2_f32_bf16 v94, v126, s54, v46\n\t"
      "v_dot2_f32_bf16 v95, v127, s54, v47\n\t"
      "DD111_%=:\n\t"
      "v_add_u32_e32 v26, v31, %[eb]\n\t"
      "s_nop 1\n\t"
      "v_mfma_f32_32x32x16_bf16 v[80:95], v[60:63], v[56:59], v[80:95]\n\t"
      "ds_read_b128 v[60:63], v26\n\t"
      "ds_add_rtn_u32 v30, %[nxa], v24\n\t"
      "s_bfe_u32 s52, s56, 0x50018\n\t"
      "v_lshl_add_u32 v26, s52, 3, %[gnb]\n\t"
      "ds_read_b32 v29, v26\n\t"
      "v_lshl_add_u32 v26, s48, 5, %[qb]\n\t"
      "v_lshl_add_u32 v25, s48, 4, %[eib]\n\t"
      "ds_read_b128 v[64:67], v26 offset:0\n\t"
      "ds_read_b128 v[68:71], v26 offset:32\n\t"
      "ds_read_b128 v[72:75], v26 offset:64\n\t"
      "ds_read_b128 v[76:79], v26 offset:96\n\t"
      "ds_read_u16 v31, v25\n\t"
      "ds_read_u16_d16_hi v38, v34 offset:%c[uo]\n\t"
      "ds_read_u16_d16_hi v39, v35 offset:%c[uo]\n\t"
      "ds_read_u16_d16_hi v36, v34\n\t"
      "ds_read_u16_d16_hi v37, v35\n\t"
      "v_exp_f32_e64 v80, v80 clamp\n\t"
      "v_exp_f32_e64 v81, v81 clamp\n\t"
      "v_exp_f32_e64 v82, v82 clamp\n\t"
      "v_exp_f32_e64 v83, v83 clamp\n\t"
      "v_pk_fma_f32 v[80:81], v[80:81], s[68:69], v[40:41] op_sel_hi:[1,0,0]\n\t"
      "v_pk_fma_f32 v[82:83], v[82:83], s[68:69], v[40:41] op_sel_hi:[1,0,0]\n\t"
      "v_pk_mul_f32 v[50:51], v[114:115], v[80:81]\n\t"
      "v_pk_mul_f32 v[48:49], v[80:81], v[82:83]\n\t"
      "v_pk_add_f32 v[52:53], v[80:81], v[82:83]\n\t"
      "v_rcp_f32_e32 v48, v48\n\t"
      "v_rcp_f32_e32 v49, v49\n\t"
      "v_pk_fma_f32 v[50:51], v[112:113], v[82:83], v[50:51]\n\t"
      "v_pk_fma_f32 v[42:43], v[52:53], v[48:49], v[42:43]\n\t"
      "v_pk_fma_f32 v[44:45], v[50:51], v[48:49], v[44:45]\n\t"
      "s_cmp_eq_u32 s59, 1\n\t"
      "s_cbranch_scc1 SD111_%=\n\t"
      "v_exp_f32_e64 v84, v84 clamp\n\t"
      "v_exp_f32_e64 v85, v85 clamp\n\t"
      "v_exp_f32_e64 v86, v86 clamp\n\t"
      "v_exp_f32_e64 v87, v87 clamp\n\t"
      "v_pk_fma_f32 v[84:85], v[84:85], s[68:69], v[40:41] op_sel_hi:[1,0,0]\n\t"
      "v_pk_fma_f32 v[86:87], v[86:87], s[68:69], v[40:41] op_sel_hi:[1,0,0]\n\t"
      "v_pk_mul_f32 v[50:51], v[118:119], v[84:85]\n\t"
      "v_pk_mul_f32 v[48:49], v[84:85], v[86:87]\n\t"
      "v_pk_add_f32 v[52:53], v[84:85], v[86:87]\n\t"
      "v_rcp_f32_e32 v48, v48\n\t"
      "v_rcp_f32_e32 v49, v49\n\t"
      "v_pk_fma_f32 v[50:51], v[116:117], v[86:87], v[50:51]\n\t"
      "v_pk_fma_f32 v[42:43], v[52:53], v[48:49], v[42:43]\n\t"
      "v_pk_fma_f32 v[44:45], v[50:51], v[48:49], v[44:45]\n\t"
      "s_cmp_eq_u32 s59, 2\n\t"
      "s_cbranch_scc1 SD111_%=\n\t"
      "v_exp_f32_e64 v88, v88 clamp\n\t"
      "v_exp_f32_e64 v89, v89 clamp\n\t"
      "v_exp_f32_e64 v90, v90 clamp\n\t"
      "v_exp_f32_e64 v91, v91 clamp\n\t"
      "v_pk_fma_f32 v[88:89], v[88:89], s[68:69], v[40:41] op_sel_hi:[1,0,0]\n\t"
      "v_pk_fma_f32 v[90:91], v[90:91], s[68:69], v[40:41] op_sel_hi:[1,0,0]\n\t"
      "v_pk_mul_f32 v[50:51], v[122:123], v[88:89]\n\t"
      "v_pk_mul_f32 v[48:49], v[88:89], v[90:91]\n\t"
      "v_pk_add_f32 v[52:53], v[88:89], v[90:91]\n\t"
      "v_rcp_f32_e32 v48, v48\n\t"
      "v_rcp_f32_e32 v49, v49\n\t"
      "v_pk_fma_f32 v[50:51], v[120:121], v[90:91], v[50:51]\n\t"
      "v_pk_fma_f32 v[42:43], v[52:53], v[48:49], v[42:43]\n\t"
      "v_pk_fma_f32 v[44:45], v[50:51], v[48:49], v[44:45]\n\t"
      "s_cmp_eq_u32 s59, 3\n\t"
      "s_cbranch_scc1 SD111_%=\n\t"
      "v_exp_f32_e64 v92, v92 clamp\n\t"
      "v_exp_f32_e64 v93, v93 clamp\n\t"
      "v_exp_f32_e64 v94, v94 clamp\n\t"
      "v_exp_f32_e64 v95, v95 clamp\n\t"
      "v_pk_fma_f32 v[92:93], v[92:93], s[68:69], v[40:41] op_sel_hi:[1,0,0]\n\t"
      "v_pk_fma_f32 v[94:95], v[94:95], s[68:69], v[40:41] op_sel_hi:[1,0,0]\n\t"
      "v_pk_mul_f32 v[50:51], v[126:127], v[92:93]\n\t"
      "v_pk_mul_f32 v[48:49], v[92:93], v[94:95]\n\t"
      "v_pk_add_f32 v[52:53], v[92:93], v[94:95]\n\t"
      "v_rcp_f32_e32 v48, v48\n\t"
      "v_rcp_f32_e32 v49, v49\n\t"
      "v_pk_fma_f32 v[50:51], v[124:125], v[94:95], v[50:51]\n\t"
      "v_pk_fma_f32 v[42:43], v[52:53], v[48:49], v[42:43]\n\t"
      "v_pk_fma_f32 v[44:45], v[50:51], v[48:49], v[44:45]\n\t"
      "SD111_%=:\n\t"
      "s_waitcnt lgkmcnt(0)\n\t"
      "ds_read_b32 v30, v30\n\t"
      "s_bfe_u32 s75, s57, 0x50018\n\t"
      "v_rcp_f32_e32 v48, v42\n\t"
      "v_rcp_f32_e32 v49, v43\n\t"
      "v_mov_b64_e32 v[42:43], s[72:73]\n\t"
      "v_pk_fma_f32 v[50:51], v[44:45], v[48:49], v[38:39]\n\t"
      "v_mov_b64_e32 v[44:45], 0\n\t"
      "v_max_f32_e32 v50, 0, v50\n\t"
      "v_max_f32_e32 v51, 0, v51\n\t"
      "v_lshl_add_u32 v26, s75, 2, %[gcb]\n\t"
      "v_pk_add_f32 v[50:51], v[50:51], v[36:37]\n\t"
      "s_cmp_gt_u32 s56, s74\n\t"
      "v_cvt_pk_bf16_f32 v52, v50, v51\n\t"
      "ds_write_b16 v34, v52 offset:%c[ao]\n\t"
      "ds_write_b16_d16_hi v35, v52 offset:%c[ao]\n\t"
      "ds_add_rtn_u32 v27, v26, v24\n\t"
      "s_cbranch_scc1 PR111_%=\n\t"
      "PB111_%=:\n\t"
      "v_xor_b32_sdwa v34, v29, v32 dst_sel:DWORD dst_unused:UNUSED_PAD src0_sel:WORD_0 src1_sel:DWORD\n\t"
      "v_xor_b32_sdwa v35, v29, v32 dst_sel:DWORD dst_unused:UNUSED_PAD src0_sel:WORD_1 src1_sel:DWORD\n\t"
      "ds_read_u16_d16_hi v46, v34 offset:%c[ao]\n\t"
      "ds_read_u16_d16_hi v47, v35 offset:%c[ao]\n\t"
      "T111_%=:\n\t"
      "s_mov_b32 s57, s50\n\t"
      "s_mov_b32 s59, s49\n\t"
      "s_mov_b32 s66, B110_%=-PC_%=\n\t"
      "s_mov_b32 s67, B111_%=-PC_%=\n\t"
      "s_cmp_eq_u32 s49, 0\n\t"
      "s_cbranch_scc1 X0111_%=\n\t"
      "s_waitcnt vmcnt(0)\n\t"
      "buffer_load_dword v112, v64, s[40:43], 0 offen sc1\n\t"
      "buffer_load_dword v113, v65, s[40:43], 0 offen sc1\n\t"
      "buffer_load_dword v114, v66, s[40:43], 0 offen sc1\n\t"
      "buffer_load_dword v115, v67, s[40:43], 0 offen sc1\n\t"
      "s_cmp_eq_u32 s49, 1\n\t"
      "s_cbranch_scc1 XO111_%=\n\t"
      "buffer_load_dword v116, v68, s[40:43], 0 offen sc1\n\t"
      "buffer_load_dword v117, v69, s[40:43], 0 offen sc1\n\t"
      "buffer_load_dword v118, v70, s[40:43], 0 offen sc1\n\t"
      "buffer_load_dword v119, v71, s[40:43], 0 offen sc1\n\t"
      "s_cmp_eq_u32 s49, 2\n\t"
      "s_cbranch_scc1 XO111_%=\n\t"
      "buffer_load_dword v120, v72, s[40:43], 0 offen sc1\n\t"
      "buffer_load_dword v121, v73, s[40:43], 0 offen sc1\n\t"
      "buffer_load_dword v122, v74, s[40:43], 0 offen sc1\n\t"
      "buffer_load_dword v123, v75, s[40:43], 0 offen sc1\n\t"
      "s_cmp_eq_u32 s49, 3\n\t"
      "s_cbranch_scc1 XO111_%=\n\t"
      "buffer_load_dword v124, v76, s[40:43], 0 offen sc1\n\t"
      "buffer_load_dword v125, v77, s[40:43], 0 offen sc1\n\t"
      "buffer_load_dword v126, v78, s[40:43], 0 offen sc1\n\t"
      "buffer_load_dword v127, v79, s[40:43], 0 offen sc1\n\t"
      "XO111_%=:\n\t"
      "s_waitcnt lgkmcnt(0)\n\t"
      "v_readfirstlane_b32 s50, v30\n\t"
      "s_and_b32 s48, s50, 0x3ff\n\t"
      "s_bfe_u32 s49, s50, 0xa000a\n\t"
      "s_bfe_u32 s52, s50, 0x20014\n\t"
      "s_mul_i32 s52, s52, %c[wstr]\n\t"
      "s_add_u32 s40, s51, s52\n\t"
      "s_addc_u32 s41, s55, 0\n\t"
      "v_readfirstlane_b32 s52, v27\n\t"
      "v_pk_add_f32 v[46:47], v[46:47], s[70:71] op_sel_hi:[1,0]\n\t"
      "s_cmp_eq_u32 s52, 12\n\t"
      "s_cbranch_scc1 RP111_%=\n\t"
      "RB111_%=:\n\t"
      "s_cmp_le_u32 s49, 4\n\t"
      "s_cselect_b32 s60, s65, s64\n\t"
      "s_add_u32 s60, s60, s62\n\t"
      "s_addc_u32 s61, s63, 0\n\t"
      "s_setpc_b64 s[60:61]\n\t"
      "X0111_%=:\n\t"
      "s_mov_b32 s66, END_%=-PC_%=\n\t"
      "s_mov_b32 s67, END_%=-PC_%=\n\t"
      "s_cmp_eq_u32 s66, s64\n\t"
      "s_cbranch_scc1 XO111_%=\n\t"
      "s_waitcnt vmcnt(0)\n\t"
      "s_branch XO111_%=\n\t"
      "RP111_%=:\n\t"
      "v_lshl_add_u32 v25, s75, 4, %[g2b]\n\t"
      "ds_read_b32 v28, v25\n\t"
      "s_waitcnt lgkmcnt(0)\n\t"
      "v_xor_b32_sdwa v26, v28, %[l16] dst_sel:DWORD dst_unused:UNUSED_PAD src0_sel:WORD_0 src1_sel:DWORD\n\t"
      "v_xor_b32_sdwa v25, v28, %[l16] dst_sel:DWORD dst_unused:UNUSED_PAD src0_sel:WORD_1 src1_sel:DWORD\n\t"
      "ds_read_b128 v[48:51], v26 offset:%c[ao]\n\t"
      "s_waitcnt lgkmcnt(0)\n\t"
      "buffer_store_dwordx4 v[48:51], v25, s[44:47], 0 offen sc1\n\t"
      "s_branch RB111_%=\n\t"
      "PR111_%=:\n\t"
      "s_or_b32 s74, s56, 0x1fffffff\n\t"
      "s_lshr_b32 s52, s74, 29\n\t"
      "v_lshl_add_u32 v25, s52, 10, %[wfb]\n\t"
      "v_lshl_or_b32 v32, s52, 6, %[c2]\n\t"
      "ds_read_b128 v[56:59], v25\n\t"
      "s_setprio 2\n\t"
      "s_cmp_eq_u32 s52, 1\n\t"
      "s_cbranch_scc1 PB111_%=\n\t"
      "s_setprio 1\n\t"
      "s_cmp_eq_u32 s52, 2\n\t"
      "s_cbranch_scc1 PB111_%=\n\t"
      "s_setprio 0\n\t"
      "s_branch PB111_%=\n\t"
      "END_%=:\n\t"
      "CODE_END:\n"
      : "+{s[40:47]}"(srds)
      : [qb] "v"(L_SRC + 16 * lh), [eib] "v"(L_EI + 2 * lc), [eb] "v"(L_E + 16 * lh), [gnb] "v"(L_GN + 4 * lh), [c2] "v"(2 * lc),
        [wfb] "v"(L_WC + 16 * (ln & 63)), [g2b] "v"(L_G2 + 4 * ((ln >> 4) & 3)), [gcb] "v"(L_GC + off1), [l16] "v"(16 * (ln & 15)),
        [it0] "v"(L_IT + 4 * ((wv & 3) * QLEN + (wv >> 2))), [nxa] "v"(S_NEXT + 4 * (wv & 3) + off1),
        [uo] "i"(L_U), [ao] "i"(L_A), [wstr] "i"(WSTR), [eps] "i"(EPS_BITS)
      : "memory", "scc",
        "v24", "v25", "v26", "v27", "v28", "v29", "v30", "v31", "v32", "v33", "v34", "v35", "v36", "v37", "v38", "v39", "v40", "v41",
        "v42", "v43", "v44", "v45", "v46", "v47", "v48", "v49", "v50", "v51", "v52", "v53", "v54", "v55", "v56", "v57", "v58", "v59",
        "v60", "v61", "v62", "v63", "v64", "v65", "v66", "v67", "v68", "v69", "v70", "v71", "v72", "v73", "v74", "v75", "v76", "v77",
        "v78", "v79", "v80", "v81", "v82", "v83", "v84", "v85", "v86", "v87", "v88", "v89", "v90", "v91", "v92", "v93", "v94", "v95",
        "v96", "v97", "v98", "v99", "v100", "v101", "v102", "v103", "v104", "v105", "v106", "v107", "v108", "v109", "v110", "v111", "v112", "v113",
        "v114", "v115", "v116", "v117", "v118", "v119", "v120", "v121", "v122", "v123", "v124", "v125", "v126", "v127", "s48", "s49", "s50", "s51",
        "s52", "s53", "s54", "s55", "s56", "s57", "s58", "s59", "s60", "s61", "s62", "s63", "s64", "s65", "s66", "s67", "s68", "s69",
        "s70", "s71", "s72", "s73", "s74", "s75", "s76", "s77");

  if (__builtin_amdgcn_readfirstlane(ldsr<unsigned>(S_SLOW)) != 0) {
    int tid = threadIdx.x;
    asm volatile("" : "+v"(tid));
    const int c = tid & 31, h = (tid >> 5) & 1;
    __builtin_amdgcn_fence(__ATOMIC_ACQUIRE, "agent");
    asm volatile("s_waitcnt vmcnt(0) lgkmcnt(0)\n\ts_barrier" ::: "memory");
    const auto ro = make_rsrc(out + (size_t)node0 * 128, nn * 256);
    for (int w = 0; w < 4; ++w) {
      for (int i = tid; i < NB * 64; i += 1024) ldsw<unsigned>(L_E + 4 * i, 0u);
      bar_lds();
      const u32x4 w0 = ldsr<u32x4>(L_WC + w * 1024 + c * 16), w1 = ldsr<u32x4>(L_WC + w * 1024 + 512 + c * 16);
      for (int k = wv; k < 256; k += 16) {
        const unsigned sg = ld1_dev(sseg, seg_at(k));
        const int b0 = sg & 0x7ff, b1 = ((sg >> 22) & 511) != ldsr<unsigned>(S_TAG) ? 0 : b0 + ((sg >> 11) & 0x7ff);
        for (int i = b0 + h; i < b1; i += 2) {
          const unsigned rec = ((const unsigned*)(ws + WS_REC))[k * CH + i], node = rec & 127;
          const u32x4 e0 = *(const u32x4*)(ws + WS_EP + (k * CH + i) * 32), e1 = *(const u32x4*)(ws + WS_EP + (k * CH + i) * 32 + 16);
          const unsigned wd = *(const unsigned*)(ws + WS_TAB + (size_t)w * WSTR + (rec & ~127u) + 4 * c);
          float gs = bf2f(ldsr<uint16_t>(L_A + (rowbase(node) ^ (64 * w + 2 * c)))) + bf2f(wd);
#pragma unroll
          for (int j = 0; j < 4; ++j) {
            gs = __builtin_amdgcn_fdot2_f32_bf16(as_bf2(e0[j]), as_bf2(w0[j]), gs, false);
            gs = __builtin_amdgcn_fdot2_f32_bf16(as_bf2(e1[j]), as_bf2(w1[j]), gs, false);
          }
          const float eta = __builtin_amdgcn_rcpf(fmaf(__builtin_amdgcn_exp2f(gs), 0x1p-30f, 0x1p-30f));
          __hip_atomic_fetch_add((LAS float*)(L_E + node * 256 + 4 * c), eta * __uint_as_float(wd), __ATOMIC_RELAXED, __HIP_MEMORY_SCOPE_WORKGROUP);
          __hip_atomic_fetch_add((LAS float*)(L_E + node * 256 + 128 + 4 * c), eta, __ATOMIC_RELAXED, __HIP_MEMORY_SCOPE_WORKGROUP);
        }
      }
      bar_lds();
      for (int n = 2 * wv + h; n < NB; n += 32) {
        const unsigned o = rowbase(n) ^ (64 * w + 2 * c);
        const float z = fmaf(ldsr<float>(L_E + n * 256 + 4 * c), __builtin_amdgcn_rcpf(ldsr<float>(L_E + n * 256 + 128 + 4 * c) + EPS), bf2f(ldsr<uint16_t>(L_U + o)));
        __builtin_amdgcn_raw_buffer_store_b16((uint16_t)cvt_pk(bf2f(ldsr<uint16_t>(L_X + o)) + fmaxf(z, 0.f), 0.f), ro, n * 256 + (32 * w + c) * 2, 0, 0);
      }
      bar_lds();
    }
  }
}

extern "C" void kernel_launch(void* const* d_in, const int* in_sizes, int n_in, void* d_out, int out_size, void* d_ws, size_t ws_size,
                              hipStream_t stream) {
  gatedgcn<<<256, 1024, 0, stream>>>((const uint16_t*)d_in[0], (const uint16_t*)d_in[1], (const int*)d_in[2], (const int*)d_in[3],
                                     (const uint16_t*)d_in[4], (const uint16_t*)d_in[5], (const uint16_t*)d_in[6], (const uint16_t*)d_in[7],
                                     (const uint16_t*)d_in[8], (char*)d_ws, (uint16_t*)d_out);
}
```
